```python
import torch

def _softmax(x, axis=-1):
    return torch.softmax(x, dim=axis)
def _gelu(x):
    return torch.nn.functional.gelu(x, approximate="tanh")
def _silu(x):
    return torch.nn.functional.silu(x)
def _sigmoid(x):
    return torch.sigmoid(x)
def _layernorm(x, axis=-1, eps=1e-5):
    m = x.mean(dim=axis, keepdim=True)
    v = x.var(dim=axis, keepdim=True, unbiased=False)
    return (x - m) / torch.sqrt(v + eps)
def _scatter_add(tgt, idx, src, axis=0):
    return tgt.clone().index_add_(axis, idx, src)
def _segment_sum(src, seg, n):
    out = torch.zeros((n,) + src.shape[1:], dtype=src.dtype)
    return out.index_add_(0, seg, src)
def _pad(x, pw):
    flat = tuple(v for pair in reversed(pw) for v in pair)
    return torch.nn.functional.pad(x, flat)

kernel_name = 'recurrent_scan__ssm_conv_s5__swiglu_ffn__prompt'

def setup_inputs():

    _w = (lambda a: (lambda t: t.to(torch.bfloat16) if t.is_floating_point() else t)(torch.as_tensor(a)))
    def _A_setup():
        B, S, D = 1, 2048, 448
        P = D
        Lambda_re = -0.5 * (lambda _x: torch.exp(torch.as_tensor(_x)))((lambda *s: torch.randn(*s, dtype=torch.bfloat16) * 0.2)(P))
        Lambda_im = (lambda *s: torch.randn(*s, dtype=torch.bfloat16) * 0.4)(P) * 1.0
        B_re = (lambda *s: torch.randn(*s, dtype=torch.bfloat16) * 0.2)(P, D) * (D ** -0.5)
        B_im = (lambda *s: torch.randn(*s, dtype=torch.bfloat16) * 0.2)(P, D) * (D ** -0.5)
        C_re = (lambda *s: torch.randn(*s, dtype=torch.bfloat16) * 0.2)(D, P) * (P ** -0.5)
        C_im = (lambda *s: torch.randn(*s, dtype=torch.bfloat16) * 0.2)(D, P) * (P ** -0.5)
        log_dt = (lambda *s: torch.randn(*s, dtype=torch.bfloat16) * 0.2)(P) * 0.3 - 2.0
        Dskip = (lambda *s: torch.randn(*s, dtype=torch.bfloat16) * 0.05)(D)
        return {
            "x": _w((lambda *s: torch.randn(*s, dtype=torch.bfloat16) * 0.05)(B, S, D)),
            "Lambda_re": _w(Lambda_re),
            "Lambda_im": _w(Lambda_im),
            "B_re": _w(B_re),
            "B_im": _w(B_im),
            "C_re": _w(C_re),
            "C_im": _w(C_im),
            "log_dt": _w(log_dt),
            "Dskip": _w(Dskip),
        }
    _A_in = _A_setup()

    def _B_setup():
        B, S, D, F = 1, 2048, 448, 1792
        return {
            "x": _w((lambda *s: torch.randn(*s, dtype=torch.bfloat16) * 0.05)(B, S, D)),
            "w_gate": _w((lambda *s: torch.randn(*s, dtype=torch.bfloat16) * 0.05)(D, F)),
            "w_up": _w((lambda *s: torch.randn(*s, dtype=torch.bfloat16) * 0.05)(D, F)),
            "w_down": _w((lambda *s: torch.randn(*s, dtype=torch.bfloat16) * 0.05)(F, D)),
        }
    _B_in = _B_setup()

    return {
        "A_x": _A_in["x"],
        "A_Lambda_re": _A_in["Lambda_re"],
        "A_Lambda_im": _A_in["Lambda_im"],
        "A_B_re": _A_in["B_re"],
        "A_B_im": _A_in["B_im"],
        "A_C_re": _A_in["C_re"],
        "A_C_im": _A_in["C_im"],
        "A_log_dt": _A_in["log_dt"],
        "A_Dskip": _A_in["Dskip"],
        "B_x": _B_in["x"],
        "B_w_gate": _B_in["w_gate"],
        "B_w_up": _B_in["w_up"],
        "B_w_down": _B_in["w_down"],
    }

def reference(A_x, A_Lambda_re, A_Lambda_im, A_B_re, A_B_im, A_C_re, A_C_im, A_log_dt, A_Dskip, B_x, B_w_gate, B_w_up, B_w_down):
    def _A_ref(x, Lambda_re, Lambda_im, B_re, B_im, C_re, C_im, log_dt, Dskip):
        Bsz, L, H = x.shape
        Lam = Lambda_re + 1j * Lambda_im
        Bmat = B_re + 1j * B_im
        Cmat = C_re + 1j * C_im
        dt = torch.exp(log_dt)
        Lam_bar = torch.exp(dt * Lam)
        B_bar = ((Lam_bar - 1.0) / Lam)[:, None] * Bmat
        Bu = torch.einsum("bld,pd->blp", x + 0j, B_bar)
        h = torch.zeros_like(Bu[:, 0, :])
        hs = []
        for t in range(L):
            h = Lam_bar[None, :] * h + Bu[:, t, :]
            hs.append(h)
        hseq = torch.stack(hs, 1)
        y = 2.0 * torch.einsum("blp,dp->bld", hseq, Cmat).real + x * Dskip[None, None, :]
        return y

    def _B_ref(x, w_gate, w_up, w_down):
        g = _silu(x @ w_gate)
        u = x @ w_up
        return (g * u) @ w_down

    x = A_x
    xn = _layernorm(x, axis=-1)
    s = _A_ref(xn, A_Lambda_re, A_Lambda_im, A_B_re, A_B_im, A_C_re, A_C_im, A_log_dt, A_Dskip)
    g = _silu(_B_ref(xn, B_w_gate, B_w_up, B_w_down))
    return x + s * g
```

```cpp
#include <hip/hip_runtime.h>

typedef unsigned short u16;
typedef unsigned int u32;
typedef unsigned long long u64;
typedef short s16x4 __attribute__((ext_vector_type(4)));
typedef __bf16 bf16x8 __attribute__((ext_vector_type(8)));
typedef __bf16 bf16x2 __attribute__((ext_vector_type(2)));
typedef float f32x4 __attribute__((ext_vector_type(4)));
typedef float f32x2 __attribute__((ext_vector_type(2)));
typedef u32 u32x4 __attribute__((ext_vector_type(4)));
typedef u32 u32x2 __attribute__((ext_vector_type(2)));
#define LAS __attribute__((address_space(3)))
#define GAS __attribute__((address_space(1)))
#define CAS __attribute__((address_space(4)))
#define RLX_AGENT __ATOMIC_RELAXED, __HIP_MEMORY_SCOPE_AGENT

constexpr int SEQ = 2048, DM = 448, FF = 1792;
constexpr int KS2 = 84;
constexpr size_t WS_A2 = 0;
constexpr size_t WS_B2 = 12u << 20;
constexpr size_t WS_GROW = 16u << 20;
constexpr size_t WS_GCOL = WS_GROW + 65536;
constexpr size_t WS_GAGG = WS_GROW + 131072;
constexpr size_t WS_ARR = WS_GROW + 262144;
constexpr size_t WS_EPOCH = WS_GROW + 327680;
constexpr u32 SPIN_CAP = 1u << 18;

constexpr int STAGE = 53248, OFF_G = 32768, OFF_U = 40960, OFF_B = 49152;
constexpr int OFF_EP = 3 * STAGE;

__device__ __forceinline__ float bf2f(u32 v) { return __uint_as_float(v << 16); }
__device__ __forceinline__ float bfhi(u32 v) { return __uint_as_float(v & 0xffff0000u); }
__device__ __forceinline__ u32 pk2(float a, float b) {
  f32x2 f = {a, b};
  return __builtin_bit_cast(u32, __builtin_convertvector(f, bf16x2));
}
__device__ __forceinline__ float rbf(float a) { return bf2f(pk2(a, a) & 0xffffu); }
__device__ __forceinline__ float silu(float a) { return a * __builtin_amdgcn_rcpf(1.f + __expf(-a)); }
__device__ __forceinline__ f32x4 mfma(bf16x8 a, bf16x8 b, f32x4 c) {
  return __builtin_amdgcn_mfma_f32_16x16x32_bf16(a, b, c, 0, 0, 0);
}
struct cf { float r, i; };
__device__ __forceinline__ cf cmul(cf a, cf b) { return {a.r * b.r - a.i * b.i, a.r * b.i + a.i * b.r}; }
__device__ __forceinline__ cf cfma(cf a, cf b, cf c) {
  return {fmaf(a.r, b.r, fmaf(-a.i, b.i, c.r)), fmaf(a.r, b.i, fmaf(a.i, b.r, c.i))};
}
__device__ __forceinline__ cf csel(bool p, cf a, cf b) { return {p ? a.r : b.r, p ? a.i : b.i}; }
__device__ __forceinline__ void put_gran(char* ws, size_t off, u32 tag, u32 val) {
  __hip_atomic_store((GAS u64*)(ws + off), ((u64)tag << 32) | val, RLX_AGENT);
}
__device__ __forceinline__ u64 get_gran(const char* ws, size_t off) {
  return __hip_atomic_load((GAS u64*)(ws + off), RLX_AGENT);
}
__device__ __forceinline__ float wave_sum(float v) {
#pragma unroll
  for (int o = 32; o >= 1; o >>= 1) v += __shfl_xor(v, o);
  return v;
}
__device__ __forceinline__ float sel8(u32x4 w, int k) {
  u32 a = (k & 2) ? w[1] : w[0], b = (k & 2) ? w[3] : w[2];
  u32 c = (k & 4) ? b : a;
  return (k & 1) ? bfhi(c) : bf2f(c);
}
__device__ __forceinline__ void glds(const char* base, u32 voff, u32 lds) {
  u32 keep;
  asm volatile("s_mov_b32 %0, m0\n\ts_mov_b32 m0, %3\n\ts_nop 0\n\tglobal_load_lds_dwordx4 %1, %2\n\ts_mov_b32 m0, %0"
               : "=&s"(keep) : "v"(voff), "s"(base), "s"(lds) : "memory");
}
#define WAITV(n) asm volatile("s_waitcnt vmcnt(" #n ")" ::: "memory")

struct K1Args {
  const u16 *x, *lre, *lim, *bre, *bim, *cre, *cim, *ldt, *wg, *wu, *wd;
  char* ws;
};

__device__ __forceinline__ void k1_helper(const K1Args& a, int hid, u32 tag, char* smem) {
  const int tid = threadIdx.x, lane = tid & 63, w = tid >> 6;
  char* ws = a.ws;
  u32x4 xr[8], br[4], wr[14];
  const int lc = lane < 56 ? lane : 55;
#pragma unroll
  for (int i = 0; i < 8; ++i) xr[i] = *(const u32x4*)(a.x + (size_t)(hid * 64 + w * 8 + i) * DM + lc * 8);
  const int cchunk = tid % 14, kg = tid / 14;
  const u16* wsrc = (hid < 16 ? a.wg : a.wu) + (hid & 15) * 112 + cchunk * 8;
#pragma unroll
  for (int i = 0; i < 14; ++i) wr[i] = *(const u32x4*)(wsrc + (size_t)((kg < 32 ? kg : 31) + 32 * i) * FF);
#pragma unroll
  for (int i = 0; i < 4; ++i) {
    int rr = w + 8 * i; rr = rr < 28 ? rr : 27;
    int row = hid * 28 + rr;
    const u16* src = row < 448 ? a.bre + (size_t)row * DM : a.bim + (size_t)(row - 448) * DM;
    br[i] = *(const u32x4*)(src + lc * 8);
  }
#pragma unroll
  for (int i = 0; i < 8; ++i) {
    float s = 0.f;
#pragma unroll
    for (int e = 0; e < 4; ++e) s += bf2f(xr[i][e]) + bfhi(xr[i][e]);
    s = wave_sum(lane < 56 ? s : 0.f);
    const float mean = s * (1.f / DM);
    float v = 0.f;
#pragma unroll
    for (int e = 0; e < 4; ++e) {
      float d0 = bf2f(xr[i][e]) - mean, d1 = bfhi(xr[i][e]) - mean;
      v += d0 * d0 + d1 * d1;
    }
    v = wave_sum(lane < 56 ? v : 0.f) * (1.f / DM);
    const float sd = sqrtf(rbf(rbf(v) + 1e-5f));
    if (lane == 0) put_gran(ws, WS_GROW + (size_t)(hid * 64 + w * 8 + i) * 8, tag, pk2(mean, sd));
  }
  float* red = (float*)smem;
  {
    float cs[8];
#pragma unroll
    for (int e = 0; e < 8; ++e) cs[e] = 0.f;
#pragma unroll
    for (int i = 0; i < 14; ++i)
#pragma unroll
      for (int e = 0; e < 4; ++e) { cs[2 * e] += bf2f(wr[i][e]); cs[2 * e + 1] += bfhi(wr[i][e]); }
    if (kg < 32) {
      *(f32x4*)(red + kg * 112 + cchunk * 8) = f32x4{cs[0], cs[1], cs[2], cs[3]};
      *(f32x4*)(red + kg * 112 + cchunk * 8 + 4) = f32x4{cs[4], cs[5], cs[6], cs[7]};
    }
  }
#pragma unroll
  for (int i = 0; i < 4; ++i) {
    float s = 0.f;
#pragma unroll
    for (int e = 0; e < 4; ++e) s += bf2f(br[i][e]) + bfhi(br[i][e]);
    s = wave_sum(lane < 56 ? s : 0.f);
    const int rr = w + 8 * i, row = hid * 28 + rr;
    if (lane == 0 && rr < 28) {
      const int im = row >= 448, ch = row - 448 * im;
      put_gran(ws, WS_GCOL + (size_t)((ch >> 4) * 160 + 128 + 16 * im + (ch & 15)) * 8, tag, __float_as_uint(s));
    }
  }
  __syncthreads();
  if (tid < 112) {
    float s = 0.f;
#pragma unroll
    for (int k = 0; k < 32; ++k) s += red[k * 112 + tid];
    const int f = (hid & 15) * 112 + tid;
    put_gran(ws, WS_GCOL + (size_t)((f >> 6) * 160 + (hid < 16 ? 0 : 64) + (f & 63)) * 8, tag, __float_as_uint(s));
  }
  if (tid == 0) __hip_atomic_store((GAS u32*)(ws + WS_ARR) + blockIdx.x, tag, RLX_AGENT);
  const int gw = hid * 8 + w, n = lane & 15, q = lane >> 4;
  {
    u16 v[7][8];
#pragma unroll
    for (int it = 0; it < 7; ++it) {
      int fr = gw + 256 * it; fr = fr < 1568 ? fr : 1567;
      const int db = fr % 28, ks = fr / 28;
      const u16* src = a.wd + (size_t)(32 * ks + 4 * q) * DM + 16 * db + n;
#pragma unroll
      for (int e = 0; e < 8; ++e) v[it][e] = src[(16 * (e >> 2) + (e & 3)) * DM];
    }
    u32x2 cr[4], ci[4];
#pragma unroll
    for (int it = 0; it < 4; ++it) {
      int fr = gw + 256 * it; fr = fr < 784 ? fr : 783;
      const int g = fr % 28, db = fr / 28;
      const size_t o = (size_t)(16 * db + n) * DM + 16 * g + 4 * q;
      cr[it] = *(const u32x2*)(a.cre + o);
      ci[it] = *(const u32x2*)(a.cim + o);
    }
#pragma unroll
    for (int it = 0; it < 7; ++it) {
      const int fr = gw + 256 * it;
      if (fr < 1568) {
        const int db = fr % 28, ks = fr / 28;
        u32x4 o;
#pragma unroll
        for (int e = 0; e < 4; ++e) o[e] = (u32)v[it][2 * e] | ((u32)v[it][2 * e + 1] << 16);
        *(u32x4*)(ws + WS_B2 + ((size_t)((db >> 2) * KS2 + ks) * 4 + (db & 3)) * 1024 + lane * 16) = o;
      }
    }
#pragma unroll
    for (int it = 0; it < 4; ++it) {
      const int fr = gw + 256 * it;
      if (fr < 784) {
        const int g = fr % 28, db = fr / 28;
        u32x4 o;
        o[0] = (cr[it][0] & 0xffffu) | (ci[it][0] << 16);
        o[1] = (cr[it][0] >> 16) | (ci[it][0] & 0xffff0000u);
        o[2] = (cr[it][1] & 0xffffu) | (ci[it][1] << 16);
        o[3] = (cr[it][1] >> 16) | (ci[it][1] & 0xffff0000u);
        *(u32x4*)(ws + WS_B2 + ((size_t)((db >> 2) * KS2 + 56 + g) * 4 + (db & 3)) * 1024 + lane * 16) = o;
      }
    }
  }
  if (hid == 0) {
    if (tid < 256) {
      for (u32 spins = 0; spins < SPIN_CAP; ++spins) {
        const u32 v = __hip_atomic_load((GAS u32*)(ws + WS_ARR) + tid, RLX_AGENT);
        if (__all(v == tag)) break;
        __builtin_amdgcn_s_sleep(4);
      }
    }
    __syncthreads();
    if (tid == 0) __hip_atomic_store((GAS u32*)(ws + WS_EPOCH), tag, RLX_AGENT);
  }
}

__global__ __launch_bounds__(512, 2) void k1(K1Args a) {
  __shared__ __attribute__((aligned(16))) char smem[3 * STAGE + 4096];
  const int tid = threadIdx.x, lane = tid & 63;
  const int w = __builtin_amdgcn_readfirstlane(tid >> 6);
  const int xcd = blockIdx.x & 7, bi = blockIdx.x >> 3;
  char* ws = a.ws;
  const u32 tag = *(const CAS u32*)(ws + WS_EPOCH) + 1u;
  if (bi < 4) { k1_helper(a, bi * 8 + xcd, tag, smem); return; }
  const int chunk = 4 * (xcd & 1) + ((bi - 4) & 3), g = 7 * (xcd >> 1) + ((bi - 4) >> 2);
  const int wm = w & 3, wn = w >> 2, r = lane & 15, q = lane >> 4;

  u32 xs[4];
#pragma unroll
  for (int i = 0; i < 4; ++i) {
    const int rho = 32 * w + 8 * i + (lane >> 3), rr = rho & 15;
    const int t = chunk * 256 + (rho & 192) + 16 * (rr >> 2) + 4 * ((rho >> 4) & 3) + (rr & 3);
    xs[i] = t * (DM * 2) + 16 * ((lane & 7) ^ ((rho >> 1) & 7));
  }
  const int kr = 8 * w + (lane >> 3);
  const u32 woff = kr * (FF * 2) + g * 128 + 16 * ((lane & 7) ^ ((((kr >> 1) & 1) | (((kr >> 3) & 1) << 1)) << 1));
  const int beta = 4 * w + ((lane >> 3) & 3);
  const char* bbase = (const char*)((w & 2) ? a.bim : a.bre);
  const u32 boff = (16 * g + 8 * (beta >> 4) + (beta & 7)) * (DM * 2) + 16 * ((lane & 7) ^ ((beta >> 1) & 7));
  const u32 lds0 = (u32)(size_t)(LAS char*)smem;
#define STAGE_LOAD(s)                                                                   \
  do {                                                                                  \
    const u32 sb = lds0 + ((s) % 3) * STAGE;                                            \
    _Pragma("unroll") for (int i = 0; i < 4; ++i)                                       \
        glds((const char*)a.x + (s) * 128, xs[i], sb + (4 * w + i) * 1024);             \
    glds((const char*)a.wg + (s) * 64 * FF * 2, woff, sb + OFF_G + w * 1024);           \
    glds((const char*)a.wu + (s) * 64 * FF * 2, woff, sb + OFF_U + w * 1024);           \
    if (lane < 32) glds(bbase + (s) * 128, boff, sb + OFF_B + w * 512);                 \
  } while (0)
  STAGE_LOAD(0); STAGE_LOAD(1); STAGE_LOAD(2);

  cf lam, l8, l16, l32, l64, l128, l256, coef;
  {
    const int co = (16 * g + 8 * wn) * 2;
    const u32x4 wr_ = *(const CAS u32x4*)((const char*)a.lre + co);
    const u32x4 wi_ = *(const CAS u32x4*)((const char*)a.lim + co);
    const u32x4 wt_ = *(const CAS u32x4*)((const char*)a.ldt + co);
    const float lr = sel8(wr_, lane & 7), li = sel8(wi_, lane & 7);
    const float dt = rbf(expf(sel8(wt_, lane & 7)));
    const float e = expf(dt * lr);
    float sn, cs;
    sincosf(dt * li, &sn, &cs);
    lam = {e * cs, e * sn};
    const float nr = lam.r - 1.f, ni = lam.i, den = 1.f / (lr * lr + li * li);
    coef = {(nr * lr + ni * li) * den, (ni * lr - nr * li) * den};
    cf t2 = cmul(lam, lam), t4 = cmul(t2, t2);
    l8 = cmul(t4, t4); l16 = cmul(l8, l8); l32 = cmul(l16, l16); l64 = cmul(l32, l32);
    l128 = cmul(l64, l64); l256 = cmul(l128, l128);
  }

  const int xo0 = (64 * wm + r) * 128 + 16 * ((0 + q) ^ ((r >> 1) & 7));
  const int xo1 = (64 * wm + r) * 128 + 16 * ((4 + q) ^ ((r >> 1) & 7));
  const int sw2 = ((r >> 3) & 1) | ((q & 1) << 1);
  const int wo0 = OFF_G + (8 * q + (r >> 2)) * 128 + 32 * ((2 * wn) ^ sw2) + 8 * (r & 3);
  const int wo1 = OFF_G + (8 * q + (r >> 2)) * 128 + 32 * ((2 * wn + 1) ^ sw2) + 8 * (r & 3);
  const int bo0 = OFF_B + (16 * wn + r) * 128 + 16 * ((0 + q) ^ ((r >> 1) & 7));
  const int bo1 = OFF_B + (16 * wn + r) * 128 + 16 * ((4 + q) ^ ((r >> 1) & 7));

  f32x4 accG[2][4], accU[2][4], accB[4];
#pragma unroll
  for (int m = 0; m < 4; ++m) {
    accG[0][m] = accG[1][m] = accU[0][m] = accU[1][m] = accB[m] = f32x4{0.f, 0.f, 0.f, 0.f};
  }
  u64 gr = 0;
  const size_t goff = tid < 256 ? WS_GROW + (size_t)(chunk * 256 + tid) * 8
                                : WS_GCOL + (size_t)(g * 160 + (tid < 416 ? tid - 256 : 0)) * 8;
#define TR(o) __builtin_amdgcn_ds_read_tr16_b64_v4i16((LAS s16x4*)(sb + (o)))
#define WFRAG(o) __builtin_bit_cast(bf16x8, __builtin_shufflevector(TR(o), TR((o) + 512), 0, 1, 2, 3, 4, 5, 6, 7))
#define COMPUTE(s)                                                                      \
  do {                                                                                  \
    char* sb = smem + ((s) % 3) * STAGE;                                                \
    _Pragma("unroll") for (int kk = 0; kk < 2; ++kk) {                                  \
      bf16x8 xf[4];                                                                     \
      _Pragma("unroll") for (int m = 0; m < 4; ++m)                                     \
          xf[m] = *(const bf16x8*)(sb + (kk ? xo1 : xo0) + m * 2048);                   \
      const bf16x8 g0 = WFRAG(wo0 + kk * 4096), g1 = WFRAG(wo1 + kk * 4096);            \
      const bf16x8 u0 = WFRAG(wo0 + kk * 4096 + 8192), u1 = WFRAG(wo1 + kk * 4096 + 8192); \
      const bf16x8 bf = *(const bf16x8*)(sb + (kk ? bo1 : bo0));                        \
      _Pragma("unroll") for (int m = 0; m < 4; ++m) {                                   \
        accG[0][m] = mfma(g0, xf[m], accG[0][m]);                                       \
        accG[1][m] = mfma(g1, xf[m], accG[1][m]);                                       \
        accU[0][m] = mfma(u0, xf[m], accU[0][m]);                                       \
        accU[1][m] = mfma(u1, xf[m], accU[1][m]);                                       \
        accB[m] = mfma(xf[m], bf, accB[m]);                                             \
      }                                                                                 \
    }                                                                                   \
  } while (0)
#define BAR() do { asm volatile("s_waitcnt lgkmcnt(0)" ::: "memory"); __builtin_amdgcn_s_barrier(); asm volatile("" ::: "memory"); } while (0)
  WAITV(14); BAR(); COMPUTE(0);
  WAITV(7); BAR(); STAGE_LOAD(3); COMPUTE(1);
  WAITV(7); BAR(); STAGE_LOAD(4); COMPUTE(2);
  WAITV(7); BAR(); STAGE_LOAD(5); COMPUTE(3);
  WAITV(7); BAR(); STAGE_LOAD(6); COMPUTE(4);
  WAITV(7); BAR(); COMPUTE(5);
  WAITV(0); BAR();
  gr = get_gran(ws, goff);
  if (tid == 0) __hip_atomic_store((GAS u32*)(ws + WS_ARR) + blockIdx.x, tag, RLX_AGENT);
  COMPUTE(6);

  f32x2* rowst = (f32x2*)(smem + OFF_EP);
  float* colsum = (float*)(smem + OFF_EP + 2048);
  f32x2* wtot = (f32x2*)(smem + OFF_EP + 2816);
  for (u32 spins = 0; spins < SPIN_CAP; ++spins) {
    if (__all((u32)(gr >> 32) == tag)) break;
    __builtin_amdgcn_s_sleep(2);
    gr = get_gran(ws, goff);
  }
  if (tid < 256) rowst[tid] = f32x2{bf2f((u32)gr & 0xffffu), 1.f / bfhi((u32)gr)};
  else if (tid < 416) colsum[tid - 256] = __uint_as_float((u32)gr);
  __syncthreads();

  const int seg = lane >> 3, hf = seg & 1, ch = 8 * wn + (lane & 7);
  cf h[8];
  {
    const float csr = colsum[128 + ch], csi = colsum[144 + ch];
#pragma unroll
    for (int i = 0; i < 8; ++i) {
      const float lo = accB[i >> 2][i & 3], hi = accB[2 + (i >> 2)][i & 3];
      const float snd = hf ? lo : hi, keep = hf ? hi : lo;
      const float rcv = __builtin_bit_cast(float, __builtin_amdgcn_update_dpp(0, __builtin_bit_cast(int, snd), 0x128, 0xf, 0xf, false));
      const f32x2 st = rowst[64 * wm + 8 * seg + i];
      const float br = ((hf ? rcv : keep) - st[0] * csr) * st[1];
      const float bim = ((hf ? keep : rcv) - st[0] * csi) * st[1];
      const cf u = cmul(coef, cf{br, bim});
      h[i] = i ? cfma(lam, h[i - 1], u) : u;
    }
  }
  cf e = h[7];
  {
    cf p = {__shfl_up(e.r, 8), __shfl_up(e.i, 8)};
    e = csel(seg >= 1, cfma(l8, p, e), e);
    p = {__shfl_up(e.r, 16), __shfl_up(e.i, 16)};
    e = csel(seg >= 2, cfma(l16, p, e), e);
    p = {__shfl_up(e.r, 32), __shfl_up(e.i, 32)};
    e = csel(seg >= 4, cfma(l32, p, e), e);
  }
  cf pre = {__shfl_up(e.r, 8), __shfl_up(e.i, 8)};
  pre = csel(seg >= 1, pre, cf{0.f, 0.f});
  if (seg == 7) wtot[wm * 16 + ch] = f32x2{e.r, e.i};
  __syncthreads();
  cf cw = {0.f, 0.f};
#pragma unroll
  for (int k = 0; k < 3; ++k) {
    const f32x2 v = wtot[k * 16 + ch];
    if (k < wm) cw = cfma(l64, cw, cf{v[0], v[1]});
  }
  const size_t aoff = WS_GAGG + (size_t)(g * 8 * 16) * 16;
  if (wm == 3 && seg == 7 && chunk < 7) {
    const cf agg = cfma(l64, cw, e);
    put_gran(ws, aoff + (size_t)(chunk * 16 + ch) * 16, tag, __float_as_uint(agg.r));
    put_gran(ws, aoff + (size_t)(chunk * 16 + ch) * 16 + 8, tag, __float_as_uint(agg.i));
  }
  char* a2 = ws + WS_A2 + (size_t)(chunk * 4 + wm) * KS2 * 4096;
  {
    f32x4 csg[2], csu[2];
#pragma unroll
    for (int nb = 0; nb < 2; ++nb) {
      csg[nb] = *(const f32x4*)(colsum + 32 * wn + 16 * nb + 4 * q);
      csu[nb] = *(const f32x4*)(colsum + 64 + 32 * wn + 16 * nb + 4 * q);
    }
#pragma unroll
    for (int m = 0; m < 4; ++m) {
      const f32x2 st = rowst[64 * wm + 16 * (r >> 2) + 4 * m + (r & 3)];
      float hv[8];
#pragma unroll
      for (int nb = 0; nb < 2; ++nb)
#pragma unroll
        for (int j = 0; j < 4; ++j) {
          const float gv = (accG[nb][m][j] - st[0] * csg[nb][j]) * st[1];
          const float uv = (accU[nb][m][j] - st[0] * csu[nb][j]) * st[1];
          hv[nb * 4 + j] = silu(gv) * uv;
        }
      u32x4 o = {pk2(hv[0], hv[1]), pk2(hv[2], hv[3]), pk2(hv[4], hv[5]), pk2(hv[6], hv[7])};
      *(u32x4*)(a2 + (size_t)(2 * g + wn) * 4096 + m * 1024 + lane * 16) = o;
    }
  }
  cf cc = {0.f, 0.f};
  if (chunk > 0) {
    const bool need = seg < chunk;
    const size_t po = aoff + (size_t)((need ? seg : 0) * 16 + ch) * 16;
    u64 g0 = 0, g1 = 0;
    for (u32 spins = 0; spins < SPIN_CAP; ++spins) {
      g0 = get_gran(ws, po); g1 = get_gran(ws, po + 8);
      if (__all((u32)(g0 >> 32) == tag && (u32)(g1 >> 32) == tag)) break;
      __builtin_amdgcn_s_sleep(2);
    }
    cf v = {__uint_as_float((u32)g0), __uint_as_float((u32)g1)};
    const int n = chunk - 1 - seg;
    const cf l512 = cmul(l256, l256), l1024 = cmul(l512, l512);
    v = csel(n & 1, cmul(l256, v), v);
    v = csel(n & 2, cmul(l512, v), v);
    v = csel(n & 4, cmul(l1024, v), v);
    v = csel(need, v, cf{0.f, 0.f});
#pragma unroll
    for (int o = 8; o <= 32; o <<= 1) { v.r += __shfl_xor(v.r, o); v.i += __shfl_xor(v.i, o); }
    cc = v;
  }
  {
    cc = csel(wm & 1, cmul(l64, cc), cc);
    cc = csel(wm & 2, cmul(l128, cc), cc);
    cf s = {cw.r + cc.r, cw.i + cc.i};
    s = csel(seg & 1, cmul(l8, s), s);
    s = csel(seg & 2, cmul(l16, s), s);
    s = csel(seg & 4, cmul(l32, s), s);
    s = {s.r + pre.r, s.i + pre.i};
    char* hs = a2 + (size_t)(56 + g) * 4096 + (ch >> 2) * 256 + (4 * q) * 16 + (ch & 3) * 4 + hf * 2048;
#pragma unroll
    for (int i = 0; i < 8; ++i) {
      s = cmul(lam, s);
      *(u32*)(hs + (i >> 2) * 1024 + (i & 3) * 16) = pk2(2.f * (h[i].r + s.r), -2.f * (h[i].i + s.i));
    }
  }
}

constexpr int K2W = 6, K2S = KS2 / K2W, PD = 5;
__global__ __launch_bounds__(K2W * 64) void k2(const char* __restrict__ ws, const u16* __restrict__ x,
                                               const u16* __restrict__ dsk, float* __restrict__ out) {
  __shared__ __attribute__((aligned(16))) f32x4 red[K2W * 16 * 64];
  const int tid = threadIdx.x, lane = tid & 63;
  const int w = __builtin_amdgcn_readfirstlane(tid >> 6);
  const int xcd = blockIdx.x & 7, bi = blockIdx.x >> 3;
  const int rt = 4 * xcd + (bi & 3), ct = bi >> 2;
  const int c = lane & 15, q = lane >> 4;
  const char* pa = ws + WS_A2 + (size_t)(rt * KS2 + w * K2S) * 4096 + lane * 16;
  const char* pb = ws + WS_B2 + (size_t)(ct * KS2 + w * K2S) * 4096 + lane * 16;
  bf16x8 af[PD][4], bf[PD][4];
  f32x4 acc[4][4];
#pragma unroll
  for (int n = 0; n < 4; ++n)
#pragma unroll
    for (int m = 0; m < 4; ++m) acc[n][m] = f32x4{0.f, 0.f, 0.f, 0.f};
#define K2LOAD(s)                                                              \
  do {                                                                         \
    _Pragma("unroll") for (int i = 0; i < 4; ++i) {                            \
      af[(s) % PD][i] = *(const bf16x8*)(pa + (s) * 4096 + i * 1024);          \
      bf[(s) % PD][i] = *(const bf16x8*)(pb + (s) * 4096 + i * 1024);          \
    }                                                                          \
  } while (0)
#pragma unroll
  for (int s = 0; s < PD; ++s) K2LOAD(s);
  __builtin_amdgcn_sched_barrier(0);
#pragma unroll
  for (int s = 0; s < K2S; ++s) {
#pragma unroll
    for (int n = 0; n < 4; ++n)
#pragma unroll
      for (int m = 0; m < 4; ++m) acc[n][m] = mfma(bf[s % PD][n], af[s % PD][m], acc[n][m]);
    __builtin_amdgcn_sched_barrier(0);
    if (s + PD < K2S) K2LOAD(s + PD);
    __builtin_amdgcn_sched_barrier(0);
  }
  const int mo = w & 3;
  const int t = 64 * rt + 16 * (c >> 2) + 4 * mo + (c & 3);
  u32x2 xv[4], dv[4];
#pragma unroll
  for (int n = 0; n < 4; ++n) {
    xv[n] = *(const u32x2*)(x + (size_t)t * DM + 64 * ct + 16 * n + 4 * q);
    dv[n] = *(const u32x2*)(dsk + 64 * ct + 16 * n + 4 * q);
  }
  const u32 st = *(const u32*)(ws + WS_GROW + (size_t)t * 8);
#pragma unroll
  for (int m = 0; m < 4; ++m)
    if (w >= 4 || m != w) {
#pragma unroll
      for (int n = 0; n < 4; ++n) red[(w * 16 + m * 4 + n) * 64 + lane] = acc[n][m];
    }
  __syncthreads();
  if (w < 4) {
    const float mean = bf2f(st & 0xffffu), sd = bfhi(st);
#pragma unroll
    for (int n = 0; n < 4; ++n) {
      f32x4 dn = w == 0 ? acc[n][0] : w == 1 ? acc[n][1] : w == 2 ? acc[n][2] : acc[n][3];
#pragma unroll
      for (int k = 1; k < 4; ++k) dn += red[(((w + k) & 3) * 16 + w * 4 + n) * 64 + lane];
      const f32x4 y = red[(4 * 16 + w * 4 + n) * 64 + lane] + red[(5 * 16 + w * 4 + n) * 64 + lane];
      f32x4 o;
#pragma unroll
      for (int j = 0; j < 4; ++j) {
        const float xx = (j & 1) ? bfhi(xv[n][j >> 1]) : bf2f(xv[n][j >> 1] & 0xffffu);
        const float dd = (j & 1) ? bfhi(dv[n][j >> 1]) : bf2f(dv[n][j >> 1] & 0xffffu);
        const float xn = rbf(rbf(xx - mean) / sd);
        const float s = y[j] + rbf(xn * dd);
        o[j] = xx + s * rbf(silu(rbf(dn[j])));
      }
      *(f32x4*)(out + (size_t)t * DM + 64 * ct + 16 * n + 4 * q) = o;
    }
  }
}

extern "C" void kernel_launch(void* const* d_in, const int* in_sizes, int n_in, void* d_out, int out_size,
                              void* d_ws, size_t ws_size, hipStream_t stream) {
  K1Args a;
  a.x = (const u16*)d_in[0]; a.lre = (const u16*)d_in[1]; a.lim = (const u16*)d_in[2];
  a.bre = (const u16*)d_in[3]; a.bim = (const u16*)d_in[4]; a.cre = (const u16*)d_in[5];
  a.cim = (const u16*)d_in[6]; a.ldt = (const u16*)d_in[7]; a.wg = (const u16*)d_in[10];
  a.wu = (const u16*)d_in[11]; a.wd = (const u16*)d_in[12]; a.ws = (char*)d_ws;
  k1<<<256, 512, 0, stream>>>(a);
  k2<<<224, K2W * 64, 0, stream>>>((const char*)d_ws, (const u16*)d_in[0], (const u16*)d_in[8], (float*)d_out);
}
```

```cpp
#include <hip/hip_runtime.h>

typedef unsigned short u16;
typedef unsigned int u32;
typedef unsigned long long u64;
typedef short s16x4 __attribute__((ext_vector_type(4)));
typedef __bf16 bf16x8 __attribute__((ext_vector_type(8)));
typedef __bf16 bf16x2 __attribute__((ext_vector_type(2)));
typedef float f32x4 __attribute__((ext_vector_type(4)));
typedef float f32x2 __attribute__((ext_vector_type(2)));
typedef u32 u32x4 __attribute__((ext_vector_type(4)));
typedef u32 u32x2 __attribute__((ext_vector_type(2)));
#define LAS __attribute__((address_space(3)))
#define GAS __attribute__((address_space(1)))
#define CAS __attribute__((address_space(4)))
#define RLX_AGENT __ATOMIC_RELAXED, __HIP_MEMORY_SCOPE_AGENT

constexpr int SEQ = 2048, DM = 448, FF = 1792;
constexpr int KS2 = 84;
constexpr size_t WS_A2 = 0;
constexpr size_t WS_B2 = 12u << 20;
constexpr size_t WS_GROW = 16u << 20;
constexpr size_t WS_GCOL = WS_GROW + 65536;
constexpr size_t WS_GAGG = WS_GROW + 131072;
constexpr size_t WS_ARR = WS_GROW + 262144;
constexpr size_t WS_EPOCH = WS_GROW + 327680;
#define WT_AUX 16
constexpr u32 SPIN_CAP = 1u << 18;

constexpr int STAGE = 53248, OFF_G = 32768, OFF_U = 40960, OFF_B = 49152;
constexpr int OFF_EP = 3 * STAGE;

__device__ __forceinline__ float bf2f(u32 v) { return __uint_as_float(v << 16); }
__device__ __forceinline__ float bfhi(u32 v) { return __uint_as_float(v & 0xffff0000u); }
__device__ __forceinline__ u32 pk2(float a, float b) {
  f32x2 f = {a, b};
  return __builtin_bit_cast(u32, __builtin_convertvector(f, bf16x2));
}
__device__ __forceinline__ float rbf(float a) { return bf2f(pk2(a, a) & 0xffffu); }
__device__ __forceinline__ float silu(float a) { return a * __builtin_amdgcn_rcpf(1.f + __expf(-a)); }
__device__ __forceinline__ f32x4 mfma(bf16x8 a, bf16x8 b, f32x4 c) {
  return __builtin_amdgcn_mfma_f32_16x16x32_bf16(a, b, c, 0, 0, 0);
}
struct cf { float r, i; };
__device__ __forceinline__ cf cmul(cf a, cf b) { return {a.r * b.r - a.i * b.i, a.r * b.i + a.i * b.r}; }
__device__ __forceinline__ cf cfma(cf a, cf b, cf c) {
  return {fmaf(a.r, b.r, fmaf(-a.i, b.i, c.r)), fmaf(a.r, b.i, fmaf(a.i, b.r, c.i))};
}
__device__ __forceinline__ cf csel(bool p, cf a, cf b) { return {p ? a.r : b.r, p ? a.i : b.i}; }
__device__ __forceinline__ void put_gran(char* ws, size_t off, u32 tag, u32 val) {
  __hip_atomic_store((GAS u64*)(ws + off), ((u64)tag << 32) | val, RLX_AGENT);
}
__device__ __forceinline__ u64 get_gran(const char* ws, size_t off) {
  return __hip_atomic_load((GAS u64*)(ws + off), RLX_AGENT);
}
template <int CTRL>
__device__ __forceinline__ float dpp(float v) {
  return __builtin_bit_cast(float, __builtin_amdgcn_update_dpp(0, __builtin_bit_cast(int, v), CTRL, 0xf, 0xf, false));
}
template <int CTRL>
__device__ __forceinline__ u32 dppu(u32 v) { return (u32)__builtin_amdgcn_update_dpp(0, (int)v, CTRL, 0xf, 0xf, false); }
__device__ __forceinline__ float sum8(float v) {
  v += dpp<0xB1>(v);
  v += dpp<0x4E>(v);
  v += dpp<0x141>(v);
  return v;
}
__device__ __forceinline__ void st16(char* base, u32 off, u32x4 v) {
  __builtin_amdgcn_raw_buffer_store_b128(v, __builtin_amdgcn_make_buffer_rsrc(base, 0, 0x7fffffff, 0x00020000), off, 0, WT_AUX);
}
__device__ __forceinline__ float sel8(u32x4 w, int k) {
  u32 a = (k & 2) ? w[1] : w[0], b = (k & 2) ? w[3] : w[2];
  u32 c = (k & 4) ? b : a;
  return (k & 1) ? bfhi(c) : bf2f(c);
}
__device__ __forceinline__ void glds(const char* base, u32 voff, u32 lds) {
  u32 keep;
  asm volatile("s_mov_b32 %0, m0\n\ts_mov_b32 m0, %3\n\ts_nop 0\n\tglobal_load_lds_dwordx4 %1, %2\n\ts_mov_b32 m0, %0"
               : "=&s"(keep) : "v"(voff), "s"(base), "s"(lds) : "memory");
}
__device__ __forceinline__ void glds_lo(const char* base, u32 voff, u32 lds) {
  u32 keep;
  asm volatile("s_mov_b32 %0, m0\n\ts_mov_b32 m0, %3\n\ts_mov_b32 exec_hi, 0\n\tglobal_load_lds_dwordx4 %1, %2\n\t"
               "s_mov_b32 exec_hi, -1\n\ts_mov_b32 m0, %0"
               : "=&s"(keep) : "v"(voff), "s"(base), "s"(lds) : "memory");
}
#define STAMP_DECL
#define STAMP(k, j)
#define STAMP_OUT(k)
#define WAITV(n) asm volatile("s_waitcnt vmcnt(%0)" :: "n"(n) : "memory")

struct K1Args {
  const u16 *x, *lre, *lim, *bre, *bim, *cre, *cim, *ldt, *wg, *wu, *wd;
  char* ws;
};

__device__ __forceinline__ void k1_helper(const K1Args& a, int hid, u32 tag, char* smem) {
  const int tid = threadIdx.x, lane = tid & 63, w = tid >> 6;
  char* ws = a.ws;
  STAMP_DECL; STAMP(0, 0);
  u32x4 xr[7], br[7], wr[14];
  const int row = hid * 64 + w * 8 + (lane >> 3);
#pragma unroll
  for (int i = 0; i < 7; ++i) xr[i] = *(const u32x4*)(a.x + (size_t)row * DM + (lane & 7) * 8 + 64 * i);
  const int cchunk = tid % 14, kg = tid / 14;
  const u16* wsrc = (hid < 16 ? a.wg : a.wu) + (hid & 15) * 112 + cchunk * 8;
#pragma unroll
  for (int i = 0; i < 14; ++i) wr[i] = *(const u32x4*)(wsrc + (size_t)((kg < 32 ? kg : 31) + 32 * i) * FF);
  const int rr = (w & 3) * 8 + (lane >> 3), ch = (hid & 15) * 28 + (rr < 28 ? rr : 27);
#pragma unroll
  for (int i = 0; i < 7; ++i)
    br[i] = *(const u32x4*)((hid < 16 ? a.bre : a.bim) + (size_t)ch * DM + (lane & 7) * 8 + 64 * i);
  {
    float s = 0.f;
#pragma unroll
    for (int i = 0; i < 7; ++i)
#pragma unroll
      for (int e = 0; e < 4; ++e) s += bf2f(xr[i][e]) + bfhi(xr[i][e]);
    const float mean = sum8(s) * (1.f / DM);
    float v = 0.f;
#pragma unroll
    for (int i = 0; i < 7; ++i)
#pragma unroll
      for (int e = 0; e < 4; ++e) {
        const float d0 = bf2f(xr[i][e]) - mean, d1 = bfhi(xr[i][e]) - mean;
        v += d0 * d0 + d1 * d1;
      }
    v = sum8(v) * (1.f / DM);
    const float sd = sqrtf(rbf(rbf(v) + 1e-5f));
    if ((lane & 7) == 0) put_gran(ws, WS_GROW + (size_t)row * 8, tag, pk2(mean, sd));
  }
  STAMP(0, 1);
  float* red = (float*)smem;
  {
    float cs[8];
#pragma unroll
    for (int e = 0; e < 8; ++e) cs[e] = 0.f;
#pragma unroll
    for (int i = 0; i < 14; ++i)
#pragma unroll
      for (int e = 0; e < 4; ++e) { cs[2 * e] += bf2f(wr[i][e]); cs[2 * e + 1] += bfhi(wr[i][e]); }
    if (kg < 32) {
      *(f32x4*)(red + kg * 112 + cchunk * 8) = f32x4{cs[0], cs[1], cs[2], cs[3]};
      *(f32x4*)(red + kg * 112 + cchunk * 8 + 4) = f32x4{cs[4], cs[5], cs[6], cs[7]};
    }
  }
  {
    float s = 0.f;
#pragma unroll
    for (int i = 0; i < 7; ++i)
#pragma unroll
      for (int e = 0; e < 4; ++e) s += bf2f(br[i][e]) + bfhi(br[i][e]);
    s = sum8(s);
    if ((lane & 7) == 0 && w < 4 && rr < 28)
      put_gran(ws, WS_GCOL + (size_t)((ch >> 4) * 160 + (hid < 16 ? 128 : 144) + (ch & 15)) * 8, tag, __float_as_uint(s));
  }
  __syncthreads();
  if (tid < 112) {
    float s = 0.f;
#pragma unroll
    for (int k = 0; k < 32; ++k) s += red[k * 112 + tid];
    const int f = (hid & 15) * 112 + tid;
    put_gran(ws, WS_GCOL + (size_t)((f >> 6) * 160 + (hid < 16 ? 0 : 64) + (f & 63)) * 8, tag, __float_as_uint(s));
  }
  if (tid == 0) __hip_atomic_store((GAS u32*)(ws + WS_ARR) + blockIdx.x, tag, RLX_AGENT);
  STAMP(0, 2);
  const int gw = hid * 8 + w, n = lane & 15, q = lane >> 4;
  {
    u16 v[7][8];
#pragma unroll
    for (int it = 0; it < 7; ++it) {
      int fr = gw + 256 * it; fr = fr < 1568 ? fr : 1567;
      const int db = fr % 28, ks = fr / 28;
      const u16* src = a.wd + (size_t)(32 * ks + 4 * q) * DM + 16 * db + n;
#pragma unroll
      for (int e = 0; e < 8; ++e) v[it][e] = src[(16 * (e >> 2) + (e & 3)) * DM];
    }
    u32x2 cr[4], ci[4];
#pragma unroll
    for (int it = 0; it < 4; ++it) {
      int fr = gw + 256 * it; fr = fr < 784 ? fr : 783;
      const int g = fr % 28, db = fr / 28;
      const size_t o = (size_t)(16 * db + n) * DM + 16 * g + 4 * q;
      cr[it] = *(const u32x2*)(a.cre + o);
      ci[it] = *(const u32x2*)(a.cim + o);
    }
#pragma unroll
    for (int it = 0; it < 7; ++it) {
      const int fr = gw + 256 * it;
      if (fr < 1568) {
        const int db = fr % 28, ks = fr / 28;
        u32x4 o;
#pragma unroll
        for (int e = 0; e < 4; ++e) o[e] = (u32)v[it][2 * e] | ((u32)v[it][2 * e + 1] << 16);
        st16(ws + WS_B2, (((db >> 2) * KS2 + ks) * 4 + (db & 3)) * 1024 + lane * 16, o);
      }
    }
#pragma unroll
    for (int it = 0; it < 4; ++it) {
      const int fr = gw + 256 * it;
      if (fr < 784) {
        const int g = fr % 28, db = fr / 28;
        u32x4 o;
        o[0] = (cr[it][0] & 0xffffu) | (ci[it][0] << 16);
        o[1] = (cr[it][0] >> 16) | (ci[it][0] & 0xffff0000u);
        o[2] = (cr[it][1] & 0xffffu) | (ci[it][1] << 16);
        o[3] = (cr[it][1] >> 16) | (ci[it][1] & 0xffff0000u);
        st16(ws + WS_B2, (((db >> 2) * KS2 + 56 + g) * 4 + (db & 3)) * 1024 + lane * 16, o);
      }
    }
  }
  STAMP(0, 3);
  if (hid == 0) {
    if (tid < 256) {
      for (u32 spins = 0; spins < SPIN_CAP; ++spins) {
        const u32 v = __hip_atomic_load((GAS u32*)(ws + WS_ARR) + tid, RLX_AGENT);
        if (__all(v == tag)) break;
        __builtin_amdgcn_s_sleep(4);
      }
    }
    __syncthreads();
    if (tid == 0) __hip_atomic_store((GAS u32*)(ws + WS_EPOCH), tag, RLX_AGENT);
    STAMP(0, 4);
  }
  STAMP_OUT(0);
}

__global__ __launch_bounds__(512, 2) void k1(K1Args a) {
  __shared__ __attribute__((aligned(16))) char smem[3 * STAGE + 4096];
  const int tid = threadIdx.x, lane = tid & 63;
  const int w = __builtin_amdgcn_readfirstlane(tid >> 6);
  const int xcd = blockIdx.x & 7, bi = blockIdx.x >> 3;
  char* ws = a.ws;
  const u32 tag = *(const CAS u32*)(ws + WS_EPOCH) + 1u;
  STAMP_DECL; STAMP(0, 0);
  if (bi < 4) { k1_helper(a, bi * 8 + xcd, tag, smem); return; }
  const int chunk = 4 * (xcd & 1) + ((bi - 4) & 3), g = 7 * (xcd >> 1) + ((bi - 4) >> 2);
  const int wm = w & 3, wn = w >> 2, r = lane & 15, q = lane >> 4;

  u32 xs[4];
#pragma unroll
  for (int i = 0; i < 4; ++i) {
    const int rho = 32 * w + 8 * i + (lane >> 3), rr = rho & 15;
    const int t = chunk * 256 + (rho & 192) + 16 * (rr >> 2) + 4 * ((rho >> 4) & 3) + (rr & 3);
    xs[i] = t * (DM * 2) + 16 * ((lane & 7) ^ ((rho >> 1) & 7));
  }
  const int kr = 8 * w + (lane >> 3);
  const u32 woff = kr * (FF * 2) + g * 128 + 16 * ((lane & 7) ^ ((((kr >> 1) & 1) | (((kr >> 3) & 1) << 1)) << 1));
  const int beta = 4 * w + ((lane >> 3) & 3);
  const char* bbase = (const char*)((w & 2) ? a.bim : a.bre);
  const u32 boff = (16 * g + 8 * (beta >> 4) + (beta & 7)) * (DM * 2) + 16 * ((lane & 7) ^ ((beta >> 1) & 7));
  const u32 lds0 = (u32)(size_t)(LAS char*)smem;
#define STAGE_LOAD(s)                                                                   \
  do {                                                                                  \
    const u32 sb = lds0 + ((s) % 3) * STAGE;                                            \
    _Pragma("unroll") for (int i = 0; i < 4; ++i)                                       \
        glds((const char*)a.x + (s) * 128, xs[i], sb + (4 * w + i) * 1024);             \
    glds((const char*)a.wg + (s) * 64 * FF * 2, woff, sb + OFF_G + w * 1024);           \
    glds((const char*)a.wu + (s) * 64 * FF * 2, woff, sb + OFF_U + w * 1024);           \
    glds_lo(bbase + (s) * 128, boff, sb + OFF_B + w * 512);                             \
  } while (0)
  STAGE_LOAD(0); STAGE_LOAD(1); STAGE_LOAD(2);
  STAMP(0, 1);
#define PFN 0

  cf lam, l8, l16, l32, l64, l128, l256, coef;
  {
    const int co = (16 * g + 8 * wn) * 2;
    const u32x4 wr_ = *(const CAS u32x4*)((const char*)a.lre + co);
    const u32x4 wi_ = *(const CAS u32x4*)((const char*)a.lim + co);
    const u32x4 wt_ = *(const CAS u32x4*)((const char*)a.ldt + co);
    const float lr = sel8(wr_, lane & 7), li = sel8(wi_, lane & 7);
    const float dt = rbf(expf(sel8(wt_, lane & 7)));
    const float e = expf(dt * lr);
    float sn, cs;
    sincosf(dt * li, &sn, &cs);
    lam = {e * cs, e * sn};
    const float nr = lam.r - 1.f, ni = lam.i, den = 1.f / (lr * lr + li * li);
    coef = {(nr * lr + ni * li) * den, (ni * lr - nr * li) * den};
    cf t2 = cmul(lam, lam), t4 = cmul(t2, t2);
    l8 = cmul(t4, t4); l16 = cmul(l8, l8); l32 = cmul(l16, l16); l64 = cmul(l32, l32);
    l128 = cmul(l64, l64); l256 = cmul(l128, l128);
  }

  const int xo0 = (64 * wm + r) * 128 + 16 * ((0 + q) ^ ((r >> 1) & 7));
  const int xo1 = (64 * wm + r) * 128 + 16 * ((4 + q) ^ ((r >> 1) & 7));
  const int sw2 = ((r >> 3) & 1) | ((q & 1) << 1);
  const int wo0 = OFF_G + (8 * q + (r >> 2)) * 128 + 32 * ((2 * wn) ^ sw2) + 8 * (r & 3);
  const int wo1 = OFF_G + (8 * q + (r >> 2)) * 128 + 32 * ((2 * wn + 1) ^ sw2) + 8 * (r & 3);
  const int bo0 = OFF_B + (16 * wn + r) * 128 + 16 * ((0 + q) ^ ((r >> 1) & 7));
  const int bo1 = OFF_B + (16 * wn + r) * 128 + 16 * ((4 + q) ^ ((r >> 1) & 7));

  f32x4 accG[2][4], accU[2][4], accB[4];
#pragma unroll
  for (int m = 0; m < 4; ++m) {
    accG[0][m] = accG[1][m] = accU[0][m] = accU[1][m] = accB[m] = f32x4{0.f, 0.f, 0.f, 0.f};
  }
  u64 gr = 0;
  const size_t goff = tid < 256 ? WS_GROW + (size_t)(chunk * 256 + tid) * 8
                                : WS_GCOL + (size_t)(g * 160 + (tid < 416 ? tid - 256 : 0)) * 8;
#define TR(o) __builtin_amdgcn_ds_read_tr16_b64_v4i16((LAS s16x4*)(sb + (o)))
#define WFRAG(o) __builtin_bit_cast(bf16x8, __builtin_shufflevector(TR(o), TR((o) + 512), 0, 1, 2, 3, 4, 5, 6, 7))
  bf16x8 xf[2][4], wf[2][5];
#define FRAGS(s, kk)                                                                    \
  do {                                                                                  \
    char* sb = smem + ((s) % 3) * STAGE;                                                \
    xf[kk][0] = *(const bf16x8*)(sb + (kk ? xo1 : xo0));                                \
    wf[kk][0] = WFRAG(wo0 + kk * 4096); wf[kk][1] = WFRAG(wo1 + kk * 4096);             \
    wf[kk][2] = WFRAG(wo0 + kk * 4096 + 8192); wf[kk][3] = WFRAG(wo1 + kk * 4096 + 8192); \
    wf[kk][4] = *(const bf16x8*)(sb + (kk ? bo1 : bo0));                                \
    _Pragma("unroll") for (int m = 1; m < 4; ++m)                                       \
        xf[kk][m] = *(const bf16x8*)(sb + (kk ? xo1 : xo0) + m * 2048);                 \
  } while (0)
#define MM(kk, m)                                                                       \
  do {                                                                                  \
    accG[0][m] = mfma(wf[kk][0], xf[kk][m], accG[0][m]);                                \
    accG[1][m] = mfma(wf[kk][1], xf[kk][m], accG[1][m]);                                \
    accU[0][m] = mfma(wf[kk][2], xf[kk][m], accU[0][m]);                                \
    accU[1][m] = mfma(wf[kk][3], xf[kk][m], accU[1][m]);                                \
    accB[m] = mfma(xf[kk][m], wf[kk][4], accB[m]);                                      \
    __builtin_amdgcn_sched_barrier(0);                                                  \
  } while (0)
#define PIECE(s, i)                                                                     \
  do {                                                                                  \
    if ((s) >= 3 && (s) < 7) {                                                          \
      const u32 sb = lds0 + ((s) % 3) * STAGE;                                          \
      if ((i) < 4) glds((const char*)a.x + (s) * 128, xs[(i) & 3], sb + (4 * w + (i)) * 1024); \
      if ((i) == 4) glds((const char*)a.wg + (s) * 64 * FF * 2, woff, sb + OFF_G + w * 1024);  \
      if ((i) == 5) glds((const char*)a.wu + (s) * 64 * FF * 2, woff, sb + OFF_U + w * 1024);  \
      if ((i) == 6) glds_lo(bbase + (s) * 128, boff, sb + OFF_B + w * 512);    \
      __builtin_amdgcn_sched_barrier(0);                                                \
    }                                                                                   \
  } while (0)
#define BAR() do { asm volatile("s_waitcnt lgkmcnt(0)" ::: "memory"); __builtin_amdgcn_s_barrier(); asm volatile("" ::: "memory"); __builtin_amdgcn_sched_barrier(0); } while (0)
#define STEP(s, nv)                                                                     \
  do {                                                                                  \
    FRAGS(s, 1); __builtin_amdgcn_sched_barrier(0);                                     \
    MM(0, 0); PIECE((s) + 2, 4); MM(0, 1); PIECE((s) + 2, 5); MM(0, 2); PIECE((s) + 2, 6); MM(0, 3); \
    if ((s) < 6) { WAITV(nv); BAR(); FRAGS((s) + 1, 0); __builtin_amdgcn_sched_barrier(0); }    \
    if ((s) == 5) {                                                                     \
      gr = get_gran(ws, goff);                                                          \
      if (tid == 0) __hip_atomic_store((GAS u32*)(ws + WS_ARR) + blockIdx.x, tag, RLX_AGENT);   \
      __builtin_amdgcn_sched_barrier(0);                                                \
    }                                                                                   \
    MM(1, 0); PIECE((s) + 3, 0); MM(1, 1); PIECE((s) + 3, 1); MM(1, 2); PIECE((s) + 3, 2); MM(1, 3); PIECE((s) + 3, 3); \
  } while (0)
  STAMP(0, 3);
  WAITV(14 + PFN); BAR(); STAMP(0, 2); STAMP(0, 14); FRAGS(0, 0);
  STEP(0, 7 + PFN); STEP(1, 7 + PFN); STEP(2, 7); STEP(3, 7); STEP(4, 7); STEP(5, 0); STEP(6, 0);
  STAMP(0, 5); STAMP(0, 15);

  f32x2* rowst = (f32x2*)(smem + OFF_EP);
  float* colsum = (float*)(smem + OFF_EP + 2048);
  f32x2* wtot = (f32x2*)(smem + OFF_EP + 2816);
  for (u32 spins = 0; spins < SPIN_CAP; ++spins) {
    if (__all((u32)(gr >> 32) == tag)) break;
    __builtin_amdgcn_s_sleep(2);
    gr = get_gran(ws, goff);
  }
  STAMP(0, 6);
  if (tid < 256) rowst[tid] = f32x2{bf2f((u32)gr & 0xffffu), __builtin_amdgcn_rcpf(bfhi((u32)gr))};
  else if (tid < 416) colsum[tid - 256] = __uint_as_float((u32)gr);
  __syncthreads();

  STAMP(0, 7);
  const int seg = lane >> 3, hf = seg & 1, ch = 8 * wn + (lane & 7);
  cf h[8];
  {
    const float csr = colsum[128 + ch], csi = colsum[144 + ch];
#pragma unroll
    for (int i = 0; i < 8; ++i) {
      const float lo = accB[i >> 2][i & 3], hi = accB[2 + (i >> 2)][i & 3];
      const float snd = hf ? lo : hi, keep = hf ? hi : lo;
      const float rcv = dpp<0x128>(snd);
      const f32x2 st = rowst[64 * wm + 8 * seg + i];
      const float br = ((hf ? rcv : keep) - st[0] * csr) * st[1];
      const float bim = ((hf ? keep : rcv) - st[0] * csi) * st[1];
      const cf u = cmul(coef, cf{br, bim});
      h[i] = i ? cfma(lam, h[i - 1], u) : u;
    }
  }
  cf e = h[7];
  {
    cf p = {__shfl_up(e.r, 8), __shfl_up(e.i, 8)};
    e = csel(seg >= 1, cfma(l8, p, e), e);
    p = {__shfl_up(e.r, 16), __shfl_up(e.i, 16)};
    e = csel(seg >= 2, cfma(l16, p, e), e);
    p = {__shfl_up(e.r, 32), __shfl_up(e.i, 32)};
    e = csel(seg >= 4, cfma(l32, p, e), e);
  }
  cf pre = {__shfl_up(e.r, 8), __shfl_up(e.i, 8)};
  pre = csel(seg >= 1, pre, cf{0.f, 0.f});
  STAMP(0, 13);
  if (seg == 7) wtot[wm * 16 + ch] = f32x2{e.r, e.i};
  __syncthreads();
  cf cw = {0.f, 0.f};
#pragma unroll
  for (int k = 0; k < 3; ++k) {
    const f32x2 v = wtot[k * 16 + ch];
    if (k < wm) cw = cfma(l64, cw, cf{v[0], v[1]});
  }
  const size_t aoff = WS_GAGG + (size_t)(g * 8 * 16) * 16;
  if (wm == 3 && seg == 7 && chunk < 7) {
    const cf agg = cfma(l64, cw, e);
    put_gran(ws, aoff + (size_t)(chunk * 16 + ch) * 16, tag, __float_as_uint(agg.r));
    put_gran(ws, aoff + (size_t)(chunk * 16 + ch) * 16 + 8, tag, __float_as_uint(agg.i));
  }
  STAMP(0, 8);
  char* a2 = ws + WS_A2 + (size_t)(chunk * 4 + wm) * KS2 * 4096;
  {
    f32x4 csg[2], csu[2];
#pragma unroll
    for (int nb = 0; nb < 2; ++nb) {
      csg[nb] = *(const f32x4*)(colsum + 32 * wn + 16 * nb + 4 * q);
      csu[nb] = *(const f32x4*)(colsum + 64 + 32 * wn + 16 * nb + 4 * q);
    }
#pragma unroll
    for (int m = 0; m < 4; ++m) {
      const f32x2 st = rowst[64 * wm + 16 * (r >> 2) + 4 * m + (r & 3)];
      float hv[8];
#pragma unroll
      for (int nb = 0; nb < 2; ++nb)
#pragma unroll
        for (int j = 0; j < 4; ++j) {
          const float gv = (accG[nb][m][j] - st[0] * csg[nb][j]) * st[1];
          const float uv = (accU[nb][m][j] - st[0] * csu[nb][j]) * st[1];
          hv[nb * 4 + j] = silu(gv) * uv;
        }
      u32x4 o = {pk2(hv[0], hv[1]), pk2(hv[2], hv[3]), pk2(hv[4], hv[5]), pk2(hv[6], hv[7])};
      st16(a2, (2 * g + wn) * 4096 + m * 1024 + lane * 16, o);
    }
  }
  STAMP(0, 9);
  cf cc = {0.f, 0.f};
  if (chunk > 0) {
    const bool need = seg < chunk;
    const size_t po = aoff + (size_t)((need ? seg : 0) * 16 + ch) * 16;
    u64 g0 = 0, g1 = 0;
    for (u32 spins = 0; spins < SPIN_CAP; ++spins) {
      g0 = get_gran(ws, po); g1 = get_gran(ws, po + 8);
      if (__all((u32)(g0 >> 32) == tag && (u32)(g1 >> 32) == tag)) break;
      __builtin_amdgcn_s_sleep(2);
    }
    cf v = {__uint_as_float((u32)g0), __uint_as_float((u32)g1)};
    const int n = chunk - 1 - seg;
    const cf l512 = cmul(l256, l256), l1024 = cmul(l512, l512);
    v = csel(n & 1, cmul(l256, v), v);
    v = csel(n & 2, cmul(l512, v), v);
    v = csel(n & 4, cmul(l1024, v), v);
    v = csel(need, v, cf{0.f, 0.f});
#pragma unroll
    for (int o = 8; o <= 32; o <<= 1) { v.r += __shfl_xor(v.r, o); v.i += __shfl_xor(v.i, o); }
    cc = v;
  }
  {
    STAMP(0, 10);
    cc = csel(wm & 1, cmul(l64, cc), cc);
    cc = csel(wm & 2, cmul(l128, cc), cc);
    cf s = {cw.r + cc.r, cw.i + cc.i};
    s = csel(seg & 1, cmul(l8, s), s);
    s = csel(seg & 2, cmul(l16, s), s);
    s = csel(seg & 4, cmul(l32, s), s);
    s = {s.r + pre.r, s.i + pre.i};
    u32 o[8];
#pragma unroll
    for (int i = 0; i < 8; ++i) {
      s = cmul(lam, s);
      o[i] = pk2(2.f * (h[i].r + s.r), -2.f * (h[i].i + s.i));
    }
    const bool b0 = lane & 1, b1 = lane & 2;
#pragma unroll
    for (int k = 0; k < 8; k += 2) {
      const u32 rc = dppu<0xB1>(b0 ? o[k] : o[k + 1]);
      if (b0) o[k] = rc; else o[k + 1] = rc;
    }
#pragma unroll
    for (int k = 0; k < 8; ++k)
      if (!(k & 2)) {
        const u32 rc = dppu<0x4E>(b1 ? o[k] : o[k + 2]);
        if (b1) o[k] = rc; else o[k + 2] = rc;
      }
    const u32 ho = (56 + g) * 4096 + (ch >> 2) * 256 + (4 * q + (lane & 3)) * 16 + hf * 2048;
    st16(a2, ho, u32x4{o[0], o[1], o[2], o[3]});
    st16(a2, ho + 1024, u32x4{o[4], o[5], o[6], o[7]});
  }
  STAMP(0, 12);
  STAMP(0, 11);
  STAMP_OUT(0);
}

constexpr int K2W = 6, K2S = KS2 / K2W, PD = 5;
__global__ __launch_bounds__(K2W * 64) void k2(const char* __restrict__ ws, const u16* __restrict__ x,
                                               const u16* __restrict__ dsk, float* __restrict__ out) {
  __shared__ __attribute__((aligned(16))) f32x4 red[K2W * 16 * 64];
  const int tid = threadIdx.x, lane = tid & 63;
  const int w = __builtin_amdgcn_readfirstlane(tid >> 6);
  const int xcd = blockIdx.x & 7, bi = blockIdx.x >> 3;
  const int rt = 4 * xcd + (bi & 3), ct = bi >> 2;
  const int c = lane & 15, q = lane >> 4;
  const char* pa = ws + WS_A2 + (size_t)(rt * KS2 + w * K2S) * 4096 + lane * 16;
  const char* pb = ws + WS_B2 + (size_t)(ct * KS2 + w * K2S) * 4096 + lane * 16;
  STAMP_DECL; STAMP(1, 0);
  const int mo = w & 3;
  const int t = 64 * rt + 16 * (c >> 2) + 4 * mo + (c & 3);
  u32x2 xv[4], dv[4];
#pragma unroll
  for (int n = 0; n < 4; ++n) {
    xv[n] = *(const u32x2*)(x + (size_t)t * DM + 64 * ct + 16 * n + 4 * q);
    dv[n] = *(const u32x2*)(dsk + 64 * ct + 16 * n + 4 * q);
  }
  const u32 st = *(const u32*)(ws + WS_GROW + (size_t)t * 8);
  bf16x8 af[PD][4], bf[PD][4];
  f32x4 acc[4][4];
#pragma unroll
  for (int n = 0; n < 4; ++n)
#pragma unroll
    for (int m = 0; m < 4; ++m) acc[n][m] = f32x4{0.f, 0.f, 0.f, 0.f};
#define K2LOAD(s)                                                              \
  do {                                                                         \
    _Pragma("unroll") for (int i = 0; i < 4; ++i) {                            \
      af[(s) % PD][i] = *(const bf16x8*)(pa + (s) * 4096 + i * 1024);          \
      bf[(s) % PD][i] = *(const bf16x8*)(pb + (s) * 4096 + i * 1024);          \
    }                                                                          \
  } while (0)
#pragma unroll
  for (int s = 0; s < PD; ++s) K2LOAD(s);
  __builtin_amdgcn_sched_barrier(0);
#pragma unroll
  for (int s = 0; s < K2S; ++s) {
#pragma unroll
    for (int n = 0; n < 4; ++n)
#pragma unroll
      for (int m = 0; m < 4; ++m) acc[n][m] = mfma(bf[s % PD][n], af[s % PD][m], acc[n][m]);
    __builtin_amdgcn_sched_barrier(0);
    if (s == 0) STAMP(1, 1);
    if (s + PD < K2S) K2LOAD(s + PD);
    __builtin_amdgcn_sched_barrier(0);
  }
  STAMP(1, 3);
#pragma unroll
  for (int m = 0; m < 4; ++m)
    if (w >= 4 || m != w) {
#pragma unroll
      for (int n = 0; n < 4; ++n) red[(w * 16 + m * 4 + n) * 64 + lane] = acc[n][m];
    }
  __syncthreads();
  STAMP(1, 4);
  if (w < 4) {
    const float mean = bf2f(st & 0xffffu), rs = __builtin_amdgcn_rcpf(bfhi(st));
#pragma unroll
    for (int n = 0; n < 4; ++n) {
      f32x4 dn = w == 0 ? acc[n][0] : w == 1 ? acc[n][1] : w == 2 ? acc[n][2] : acc[n][3];
#pragma unroll
      for (int k = 1; k < 4; ++k) dn += red[(((w + k) & 3) * 16 + w * 4 + n) * 64 + lane];
      const f32x4 y = red[(4 * 16 + w * 4 + n) * 64 + lane] + red[(5 * 16 + w * 4 + n) * 64 + lane];
      f32x4 o;
#pragma unroll
      for (int j = 0; j < 4; ++j) {
        const float xx = (j & 1) ? bfhi(xv[n][j >> 1]) : bf2f(xv[n][j >> 1] & 0xffffu);
        const float dd = (j & 1) ? bfhi(dv[n][j >> 1]) : bf2f(dv[n][j >> 1] & 0xffffu);
        const float xn = rbf(rbf(xx - mean) * rs);
        const float s = y[j] + rbf(xn * dd);
        o[j] = xx + s * rbf(silu(rbf(dn[j])));
      }
      *(f32x4*)(out + (size_t)t * DM + 64 * ct + 16 * n + 4 * q) = o;
    }
  }
  STAMP(1, 5);
  STAMP_OUT(1);
}

extern "C" void kernel_launch(void* const* d_in, const int* in_sizes, int n_in, void* d_out, int out_size,
                              void* d_ws, size_t ws_size, hipStream_t stream) {
  K1Args a;
  a.x = (const u16*)d_in[0]; a.lre = (const u16*)d_in[1]; a.lim = (const u16*)d_in[2];
  a.bre = (const u16*)d_in[3]; a.bim = (const u16*)d_in[4]; a.cre = (const u16*)d_in[5];
  a.cim = (const u16*)d_in[6]; a.ldt = (const u16*)d_in[7]; a.wg = (const u16*)d_in[10];
  a.wu = (const u16*)d_in[11]; a.wd = (const u16*)d_in[12]; a.ws = (char*)d_ws;
  k1<<<256, 512, 0, stream>>>(a);
  k2<<<224, K2W * 64, 0, stream>>>((const char*)d_ws, (const u16*)d_in[0], (const u16*)d_in[8], (float*)d_out);
}
```

```cpp
#include <hip/hip_runtime.h>

typedef unsigned short u16;
typedef unsigned int u32;
typedef unsigned long long u64;
typedef short s16x4 __attribute__((ext_vector_type(4)));
typedef __bf16 bf16x8 __attribute__((ext_vector_type(8)));
typedef __bf16 bf16x2 __attribute__((ext_vector_type(2)));
typedef float f32x4 __attribute__((ext_vector_type(4)));
typedef float f32x2 __attribute__((ext_vector_type(2)));
typedef u32 u32x4 __attribute__((ext_vector_type(4)));
typedef u32 u32x2 __attribute__((ext_vector_type(2)));
#define LAS __attribute__((address_space(3)))
#define GAS __attribute__((address_space(1)))
#define CAS __attribute__((address_space(4)))
#define RLX_AGENT __ATOMIC_RELAXED, __HIP_MEMORY_SCOPE_AGENT

constexpr int SEQ = 2048, DM = 448, FF = 1792;
constexpr int KS2 = 85;
constexpr size_t WS_A2 = 4096;
constexpr size_t WS_GROW = 16u << 20;
constexpr size_t WS_GCOL = WS_GROW + 65536;
constexpr size_t WS_GAGG = WS_GROW + 131072;
constexpr size_t WS_ARR = WS_GROW + 262144;
constexpr size_t WS_EPOCH = WS_GROW + 327680;
constexpr size_t WS_FLG = WS_GROW + 393216;
#define WT_AUX 16
#define ABL_NOLOAD 0
#define PREF0 5
#define PREF1 5
#define ABL_NOTOUCH 0
#define PRIO 1
#define PIN 0
constexpr u32 SPIN_CAP = 1u << 18;

constexpr int STAGE = 53248, OFF_G = 32768, OFF_U = 40960, OFF_B = 49152;
constexpr int OFF_EP = 3 * STAGE;

__device__ __forceinline__ float bf2f(u32 v) { return __uint_as_float(v << 16); }
__device__ __forceinline__ float bfhi(u32 v) { return __uint_as_float(v & 0xffff0000u); }
__device__ __forceinline__ u32 pk2(float a, float b) {
  f32x2 f = {a, b};
  return __builtin_bit_cast(u32, __builtin_convertvector(f, bf16x2));
}
__device__ __forceinline__ float rbf(float a) { return bf2f(pk2(a, a) & 0xffffu); }
__device__ __forceinline__ float silu(float a) { return a * __builtin_amdgcn_rcpf(1.f + __expf(-a)); }
__device__ __forceinline__ f32x4 mfma(bf16x8 a, bf16x8 b, f32x4 c) {
  return __builtin_amdgcn_mfma_f32_16x16x32_bf16(a, b, c, 0, 0, 0);
}
struct cf { float r, i; };
__device__ __forceinline__ cf cmul(cf a, cf b) { return {a.r * b.r - a.i * b.i, a.r * b.i + a.i * b.r}; }
__device__ __forceinline__ cf cfma(cf a, cf b, cf c) {
  return {fmaf(a.r, b.r, fmaf(-a.i, b.i, c.r)), fmaf(a.r, b.i, fmaf(a.i, b.r, c.i))};
}
__device__ __forceinline__ cf csel(bool p, cf a, cf b) { return {p ? a.r : b.r, p ? a.i : b.i}; }
__device__ __forceinline__ void put_gran(char* ws, size_t off, u32 tag, u32 val) {
  __hip_atomic_store((GAS u64*)(ws + off), ((u64)tag << 32) | val, RLX_AGENT);
}
__device__ __forceinline__ u64 get_gran(const char* ws, size_t off) {
  return __hip_atomic_load((GAS u64*)(ws + off), RLX_AGENT);
}
template <int CTRL>
__device__ __forceinline__ float dpp(float v) {
  return __builtin_bit_cast(float, __builtin_amdgcn_update_dpp(0, __builtin_bit_cast(int, v), CTRL, 0xf, 0xf, false));
}
template <int CTRL>
__device__ __forceinline__ u32 dppu(u32 v) { return (u32)__builtin_amdgcn_update_dpp(0, (int)v, CTRL, 0xf, 0xf, false); }
__device__ __forceinline__ float sum8(float v) {
  v += dpp<0xB1>(v);
  v += dpp<0x4E>(v);
  v += dpp<0x141>(v);
  return v;
}
__device__ __forceinline__ void st16(char* base, u32 off, u32x4 v) {
  __builtin_amdgcn_raw_buffer_store_b128(v, __builtin_amdgcn_make_buffer_rsrc(base, 0, 0x7fffffff, 0x00020000), off, 0, WT_AUX);
}
__device__ __forceinline__ void glds(const char* base, u32 voff, u32 lds) {
  u32 keep;
  asm volatile("s_mov_b32 %0, m0\n\ts_mov_b32 m0, %3\n\ts_nop 0\n\tglobal_load_lds_dwordx4 %1, %2\n\ts_mov_b32 m0, %0"
               : "=&s"(keep) : "v"(voff), "s"(base), "s"(lds) : "memory");
}
__device__ __forceinline__ void glds_dev(const char* base, u32 voff, u32 lds) {
  u32 keep;
  asm volatile("s_mov_b32 %0, m0\n\ts_mov_b32 m0, %3\n\ts_nop 0\n\tglobal_load_lds_dwordx4 %1, %2 sc1\n\ts_mov_b32 m0, %0"
               : "=&s"(keep) : "v"(voff), "s"(base), "s"(lds) : "memory");
}
__device__ __forceinline__ void glds_lo(const char* base, u32 voff, u32 lds) {
  u32 keep;
  asm volatile("s_mov_b32 %0, m0\n\ts_mov_b32 m0, %3\n\ts_mov_b32 exec_hi, 0\n\tglobal_load_lds_dwordx4 %1, %2\n\t"
               "s_mov_b32 exec_hi, -1\n\ts_mov_b32 m0, %0"
               : "=&s"(keep) : "v"(voff), "s"(base), "s"(lds) : "memory");
}
#define STX 0
#define STAMP_DECL
#define STAMP(k, j)
#define STAMP_OUT(k)
#define WAITV(n) asm volatile("s_waitcnt vmcnt(%0)" :: "n"(n) : "memory")

struct K1Args {
  const u16 *x, *lre, *lim, *bre, *bim, *cre, *cim, *ldt, *wg, *wu, *wd, *dsk;
  char* ws;
  float* out;
};

__device__ __forceinline__ void k1_helper(const K1Args& a, int hid) {
  const int tid = threadIdx.x, lane = tid & 63, w = __builtin_amdgcn_readfirstlane(tid >> 6);
  char* ws = a.ws;
  STAMP_DECL; STAMP(0, 0);
  u32x4 xr[7], br[7];
  const int row = hid * 64 + w * 8 + (lane >> 3);
#pragma unroll
  for (int i = 0; i < 7; ++i) xr[i] = *(const u32x4*)(a.x + (size_t)row * DM + (lane & 7) * 8 + 64 * i);
  const int rr = (w & 3) * 8 + (lane >> 3), ch = (hid & 15) * 28 + (rr < 28 ? rr : 27);
  if (w < 4) {
#pragma unroll
    for (int i = 0; i < 7; ++i)
      br[i] = *(const u32x4*)((hid < 16 ? a.bre : a.bim) + (size_t)ch * DM + (lane & 7) * 8 + 64 * i);
  }
  const int pc = hid * 14 + (lane < 14 ? lane : 13);
  const u16 plr = a.lre[pc], pli = a.lim[pc], plt = a.ldt[pc];
  __builtin_amdgcn_sched_barrier(0);
  const u32 tag = *(const CAS u32*)(ws + WS_EPOCH) + 1u;
  {
    float s = 0.f;
#pragma unroll
    for (int i = 0; i < 7; ++i)
#pragma unroll
      for (int e = 0; e < 4; ++e) s += bf2f(xr[i][e]) + bfhi(xr[i][e]);
    const float mean = sum8(s) * (1.f / DM);
    float v = 0.f;
#pragma unroll
    for (int i = 0; i < 7; ++i)
#pragma unroll
      for (int e = 0; e < 4; ++e) {
        const float d0 = bf2f(xr[i][e]) - mean, d1 = bfhi(xr[i][e]) - mean;
        v += d0 * d0 + d1 * d1;
      }
    v = sum8(v) * (1.f / DM);
    const float sd = sqrtf(rbf(rbf(v) + 1e-5f));
    if ((lane & 7) == 0) put_gran(ws, WS_GROW + (size_t)row * 8, tag, pk2(mean, sd));
  }
  STAMP(0, 1);
  if (w < 4) {
    float s = 0.f;
#pragma unroll
    for (int i = 0; i < 7; ++i)
#pragma unroll
      for (int e = 0; e < 4; ++e) s += bf2f(br[i][e]) + bfhi(br[i][e]);
    s = sum8(s);
    if ((lane & 7) == 0 && rr < 28)
      put_gran(ws, WS_GCOL + (size_t)((ch >> 4) * 96 + (hid < 16 ? 0 : 16) + (ch & 15)) * 8, tag, __float_as_uint(s));
  }
  if (w == 7) {
    const float lr = bf2f(plr), li = bf2f(pli), dt = rbf(expf(bf2f(plt)));
    const float e = expf(dt * lr);
    float sn, cs;
    sincosf(dt * li, &sn, &cs);
    const float ar = e * cs, ai = e * sn, nr = ar - 1.f, den = 1.f / (lr * lr + li * li);
    if (lane < 14) {
      const size_t o = WS_GCOL + (size_t)((pc >> 4) * 96 + 32 + (pc & 15) * 4) * 8;
      put_gran(ws, o, tag, __float_as_uint(ar));
      put_gran(ws, o + 8, tag, __float_as_uint(ai));
      put_gran(ws, o + 16, tag, __float_as_uint((nr * lr + ai * li) * den));
      put_gran(ws, o + 24, tag, __float_as_uint((ai * lr - nr * li) * den));
    }
  }
  __syncthreads();
  if (tid == 0) __hip_atomic_store((GAS u32*)(ws + WS_ARR) + blockIdx.x, tag, RLX_AGENT);
  STAMP(0, 2);
  if (hid == 0) {
    if (tid < 256) {
#pragma unroll 1
      for (u32 spins = 0; spins < SPIN_CAP; ++spins) {
        const u32 v = __hip_atomic_load((GAS u32*)(ws + WS_ARR) + tid, RLX_AGENT);
        if (__all(v == tag)) break;
        __builtin_amdgcn_s_sleep(4);
      }
    }
    __syncthreads();
    if (tid == 0) __hip_atomic_store((GAS u32*)(ws + WS_EPOCH), tag, RLX_AGENT);
    STAMP(0, 4);
  }
  STAMP_OUT(0);
}

template <int V> struct ic { static constexpr int value = V; };
template <int I, int N, class F>
__device__ __forceinline__ void static_for(F& f) {
  if constexpr (I < N) { f(ic<I>{}); static_for<I + 1, N>(f); }
}
constexpr int NST = 21, SLOT = 32768, NSL = 5;
constexpr int NPRE = 4;
constexpr int p2_ops(int s) { return s >= NST ? 0 : s < NPRE ? 4 : 8; }
template <class F>
__device__ __forceinline__ void phase2(const K1Args& a, char* smem, u32 tag, int bi, GAS u32* myflag, F finish) {
  const int tid = threadIdx.x, lane = tid & 63;
  const int w = __builtin_amdgcn_readfirstlane(tid >> 6);
  const int xcd = blockIdx.x & 7;
  const int rt = 4 * xcd + (bi & 3), ct = bi >> 2;
  const char* ws = a.ws;
  STAMP_DECL; STAMP(1, 0);
  finish();
  const GAS u32* flg = (const GAS u32*)(ws + WS_FLG + (size_t)rt * 512) + (lane < 56 ? lane : 0);
  if (w < 4) {
    const int lw = w, rr = lane >> 3;
    const u32 lds0 = (u32)(size_t)(LAS char*)smem + lw * 4096;
    const char* pa = ws + WS_A2 + (size_t)(rt * KS2 + lw) * 4096;
    const char* pw = (const char*)a.wd + lw * 32 * (DM * 2) + ct * 128;
    const char* pc = (const char*)(lw < 2 ? a.cre : a.cim) + (64 * ct + 32 * (lw & 1)) * (DM * 2);
    const u32 ao = lane * 16;
    const u32 wo = rr * (DM * 2) + 16 * ((lane & 7) ^ (((rr >> 1) & 3) << 1));
    const u32 co0 = rr * (DM * 2) + 16 * ((lane & 7) ^ (rr >> 1)), co1 = rr * (DM * 2) + 16 * ((lane & 7) ^ (4 + (rr >> 1)));
#define ISSUE_A(s)                                                                          \
    _Pragma("unroll") for (int i = 0; i < 4; ++i)                                           \
      glds_dev(pa + (s) * 16384 + i * 1024, ao, lds0 + ((s) % NSL) * SLOT + i * 1024)
#define ISSUE_B(s)                                                                          \
    _Pragma("unroll") for (int i = 0; i < 4; ++i) {                                         \
      const u32 sb = lds0 + ((s) % NSL) * SLOT + 16384 + i * 1024;                          \
      if ((s) < 14) glds(pw + ((s) * 128 + i * 8) * (DM * 2), wo, sb);                      \
      else glds(pc + i * 8 * (DM * 2) + ((s) - 14) * 128, (i & 1) ? co1 : co0, sb);         \
    }
#pragma unroll 1
    for (u32 spins = 0; spins < SPIN_CAP; ++spins)
      if (__all(__hip_atomic_load(flg, RLX_AGENT) == tag)) break;
    WAITV(0);
    STAMP(1, 8);
    ISSUE_A(0); ISSUE_A(1); ISSUE_A(2); ISSUE_A(3); ISSUE_A(4);
    if (lane == 0) __hip_atomic_store(myflag + 64, tag, RLX_AGENT);
#pragma unroll
    for (int s = 0; s < NST; ++s) {
      WAITV(p2_ops(s + 1) + p2_ops(s + 2) + p2_ops(s + 3) + (s < 4) + (s == 0) * 4);
      __builtin_amdgcn_s_barrier();
      if (s + 4 < NST) {
        if (s > 0) { ISSUE_A(s + 4); }
        if (s + 4 >= NPRE) ISSUE_B(s + 4);
      }
    }
    STAMP_OUT(1);
    return;
  }
  u32 fs = 0;
  const int wm = w & 1, wn = (w >> 1) & 1, c = lane & 15, q = lane >> 4;
  u32x2 xv[2][2], dv[2];
  u32 st[2];
  u64 sg[2];
  WAITV(2 + STX);
  __builtin_amdgcn_sched_barrier(0);
  const int ao = 2 * wm * 1024 + lane * 16;
  const int krow = 4 * q + (c >> 2);
  int wo[2], co[4];
#pragma unroll
  for (int n = 0; n < 2; ++n) wo[n] = 16384 + krow * 128 + 32 * ((2 * wn + n) ^ ((krow >> 1) & 3)) + 8 * (c & 3);
#pragma unroll
  for (int k = 0; k < 4; ++k) co[k] = 16384 + (32 * wn + c) * 128 + 16 * ((2 * k + (q >> 1)) ^ (c >> 1)) + 8 * (q & 1);
  bf16x8 af[2][4][2], bf[2][4][2];
  f32x4 accD[2][2], accY[2][2];
#pragma unroll
  for (int n = 0; n < 2; ++n)
#pragma unroll
    for (int m = 0; m < 2; ++m) accD[n][m] = accY[n][m] = f32x4{0.f, 0.f, 0.f, 0.f};
  auto cstep = [&](auto sc) __attribute__((always_inline)) {
    constexpr int s = decltype(sc)::value;
    if (s == 5) {
      WAITV(0);
      if (lane == 0) __hip_atomic_store(myflag + 64, tag, RLX_AGENT);
      __builtin_amdgcn_sched_barrier(0);
    }
    if (s == 1) {
#pragma unroll
  for (int m = 0; m < 2; ++m) {
    const int t = 64 * rt + 16 * (c >> 2) + 4 * (2 * wm + m) + (c & 3);
    sg[m] = get_gran(ws, WS_GROW + (size_t)t * 8);
#pragma unroll
    for (int n = 0; n < 2; ++n) xv[m][n] = *(const u32x2*)(a.x + (size_t)t * DM + 64 * ct + 16 * (2 * wn + n) + 4 * q);
  }
#pragma unroll
  for (int n = 0; n < 2; ++n) dv[n] = *(const u32x2*)(a.dsk + 64 * ct + 16 * (2 * wn + n) + 4 * q);
      __builtin_amdgcn_sched_barrier(0);
    }
    if (s == 7) {
#pragma unroll
      for (int m = 0; m < 2; ++m) {
        asm volatile("" : "+v"(sg[m]));
        if (!__all((u32)(sg[m] >> 32) == tag)) {
#pragma unroll 1
          for (u32 spins = 0; spins < SPIN_CAP; ++spins) {
            sg[m] = get_gran(ws, WS_GROW + (size_t)(64 * rt + 16 * (c >> 2) + 4 * (2 * wm + m) + (c & 3)) * 8);
            if (__all((u32)(sg[m] >> 32) == tag)) break;
          }
        }
        st[m] = (u32)sg[m];
      }
      __builtin_amdgcn_sched_barrier(0);
    }
    if (s == 8) fs = __hip_atomic_load(flg + 64, RLX_AGENT);
    if (s == 10) {
#pragma unroll 1
      for (u32 spins = 0; spins < SPIN_CAP; ++spins) {
        if (__all(fs == tag)) break;
        fs = __hip_atomic_load(flg + 64, RLX_AGENT);
      }
    }
    if (s < NST) {
      asm volatile("s_waitcnt lgkmcnt(0)" ::: "memory");
      __builtin_amdgcn_s_barrier();
      asm volatile("" ::: "memory");
      if (s == 0) STAMP(1, 1);
      if (s == 7) STAMP(1, 2);
      if (s == 14) STAMP(1, 6);
      if (s == 17) STAMP(1, 7);
      if (s >= 1 && s <= 5) STAMP(1, 8 + s);
      char* sb = smem + (s % NSL) * SLOT;
#pragma unroll
      for (int k = 0; k < 4; ++k)
#pragma unroll
        for (int i = 0; i < 2; ++i) {
          af[s & 1][k][i] = *(const bf16x8*)(sb + ao + k * 4096 + i * 1024);
          s16x4 lo, hi;
          if (s < 14) {
            lo = __builtin_amdgcn_ds_read_tr16_b64_v4i16((LAS s16x4*)(sb + wo[i] + k * 4096));
            hi = __builtin_amdgcn_ds_read_tr16_b64_v4i16((LAS s16x4*)(sb + wo[i] + k * 4096 + 2048));
          } else {
            lo = *(const volatile LAS s16x4*)(sb + co[k] + i * 2048);
            hi = *(const volatile LAS s16x4*)(sb + co[k] + i * 2048 + 8192);
          }
          bf[s & 1][k][i] = __builtin_bit_cast(bf16x8, __builtin_shufflevector(lo, hi, 0, 1, 2, 3, 4, 5, 6, 7));
        }
    }
    if (s > 0) {
      const int p = (s - 1) & 1;
#pragma unroll
      for (int k = 0; k < 4; ++k)
#pragma unroll
        for (int n = 0; n < 2; ++n)
#pragma unroll
          for (int m = 0; m < 2; ++m) {
            if (s - 1 < 14) accD[n][m] = mfma(bf[p][k][n], af[p][k][m], accD[n][m]);
            else accY[n][m] = mfma(bf[p][k][n], af[p][k][m], accY[n][m]);
          }
      if (s < NST) {
#pragma unroll
        for (int i = 0; i < 12; ++i) {
          __builtin_amdgcn_sched_group_barrier(0x100, 2, 0);
          __builtin_amdgcn_sched_group_barrier(0x008, 1, 0);
        }
      }
      if (s - 1 < 14) asm volatile("" : "+v"(accD[0][0]), "+v"(accD[0][1]), "+v"(accD[1][0]), "+v"(accD[1][1]));
      else asm volatile("" : "+v"(accY[0][0]), "+v"(accY[0][1]), "+v"(accY[1][0]), "+v"(accY[1][1]));
    }
    __builtin_amdgcn_sched_barrier(0);
    if (s >= 10 && s < 14) {
      const int m = s & 1, n = (s >> 1) & 1;
      asm volatile("" : "+v"(st[m]), "+v"(xv[m][n]), "+v"(dv[n]));
      const float mean = bf2f(st[m] & 0xffffu), rs = __builtin_amdgcn_rcpf(bfhi(st[m]));
#pragma unroll
      for (int j = 0; j < 4; ++j) {
        const float xx = (j & 1) ? bfhi(xv[m][n][j >> 1]) : bf2f(xv[m][n][j >> 1] & 0xffffu);
        const float dd = (j & 1) ? bfhi(dv[n][j >> 1]) : bf2f(dv[n][j >> 1] & 0xffffu);
        accY[n][m][j] = (xx - mean) * rs * dd;
      }
      asm volatile("s_nop 1" : "+v"(accY[n][m]));
      __builtin_amdgcn_sched_barrier(0);
    }
    if (s >= 16 && s < 20) {
      const int m = s & 1, n = (s >> 1) & 1;
#pragma unroll
      for (int j = 0; j < 4; ++j) accD[n][m][j] = rbf(silu(rbf(accD[n][m][j])));
      asm volatile("s_nop 1" : "+v"(accD[n][m]));
      __builtin_amdgcn_sched_barrier(0);
    }
  };
  static_for<0, NST + 1>(cstep);
  STAMP(1, 3);
#pragma unroll
  for (int m = 0; m < 2; ++m) {
    const int t = 64 * rt + 16 * (c >> 2) + 4 * (2 * wm + m) + (c & 3);
#pragma unroll
    for (int n = 0; n < 2; ++n) {
      const f32x4 xx = {bf2f(xv[m][n][0] & 0xffffu), bfhi(xv[m][n][0]), bf2f(xv[m][n][1] & 0xffffu), bfhi(xv[m][n][1])};
      const f32x4 o = accY[n][m] * accD[n][m] + xx;
      *(f32x4*)(a.out + (size_t)t * DM + 64 * ct + 16 * (2 * wn + n) + 4 * q) = o;
    }
  }
  STAMP(1, 4);
  STAMP(1, 5);
  STAMP_OUT(1);
}

__global__ __launch_bounds__(512, 2) void k1(K1Args a) {
  __shared__ __attribute__((aligned(16))) char smem[NSL * SLOT];
  const int tid = threadIdx.x, lane = tid & 63;
  const int w = __builtin_amdgcn_readfirstlane(tid >> 6);
  const int xcd = blockIdx.x & 7, bi = blockIdx.x >> 3;
  char* ws = a.ws;
  if (bi < 4) { k1_helper(a, bi * 8 + xcd); return; }
  STAMP_DECL; STAMP(0, 0);
  const int chunk = 4 * (xcd & 1) + ((bi - 4) & 3), g = 7 * (xcd >> 1) + ((bi - 4) >> 2);
  const int wm = w & 3, wn = w >> 2, r = lane & 15, q = lane >> 4;

  u32 xs[4];
#pragma unroll
  for (int i = 0; i < 4; ++i) {
    const int rho = 32 * w + 8 * i + (lane >> 3), rr = rho & 15;
    const int t = chunk * 256 + (rho & 192) + 16 * (rr >> 2) + 4 * ((rho >> 4) & 3) + (rr & 3);
    xs[i] = t * (DM * 2) + 16 * ((lane & 7) ^ ((rho >> 1) & 7));
  }
  const int kr = 8 * w + (lane >> 3);
  const u32 woff = kr * (FF * 2) + g * 128 + 16 * ((lane & 7) ^ ((((kr >> 1) & 1) | (((kr >> 3) & 1) << 1)) << 1));
  const int beta = 4 * w + ((lane >> 3) & 3);
  const char* bbase = (const char*)((w & 2) ? a.bim : a.bre);
  const u32 boff = (16 * g + 8 * (beta >> 4) + (beta & 7)) * (DM * 2) + 16 * ((lane & 7) ^ ((beta >> 1) & 7));
  const u32 lds0 = (u32)(size_t)(LAS char*)smem;
#define STAGE_LOAD(s)                                                                   \
  do {                                                                                  \
    const u32 sb = lds0 + ((s) % 3) * STAGE;                                            \
    _Pragma("unroll") for (int i = 0; i < 4; ++i)                                       \
        glds((const char*)a.x + (s) * 128, xs[i], sb + (4 * w + i) * 1024);             \
    glds((const char*)a.wg + (s) * (64 * FF * 2), woff, sb + OFF_G + w * 1024);           \
    glds((const char*)a.wu + (s) * (64 * FF * 2), woff, sb + OFF_U + w * 1024);           \
    glds_lo(bbase + (s) * 128, boff, sb + OFF_B + w * 512);                             \
  } while (0)
  STAGE_LOAD(0); __builtin_amdgcn_s_barrier();
  STAGE_LOAD(1);
  __builtin_amdgcn_sched_barrier(0);
  STAMP(0, 1);
  const u32 tag = *(const CAS u32*)(ws + WS_EPOCH) + 1u;

  const int xo0 = (64 * wm + r) * 128 + 16 * ((0 + q) ^ ((r >> 1) & 7));
  const int xo1 = (64 * wm + r) * 128 + 16 * ((4 + q) ^ ((r >> 1) & 7));
  const int sw2 = ((r >> 3) & 1) | ((q & 1) << 1);
  const int wo0 = OFF_G + (8 * q + (r >> 2)) * 128 + 32 * ((2 * wn) ^ sw2) + 8 * (r & 3);
  const int wo1 = OFF_G + (8 * q + (r >> 2)) * 128 + 32 * ((2 * wn + 1) ^ sw2) + 8 * (r & 3);
  const int bo0 = OFF_B + (16 * wn + r) * 128 + 16 * ((0 + q) ^ ((r >> 1) & 7));
  const int bo1 = OFF_B + (16 * wn + r) * 128 + 16 * ((4 + q) ^ ((r >> 1) & 7));

  f32x4 accG[2][4], accU[2][4], accB[4];
#pragma unroll
  for (int m = 0; m < 4; ++m) {
    accG[0][m] = accG[1][m] = accU[0][m] = accU[1][m] = accB[m] = f32x4{0.f, 0.f, 0.f, 0.f};
  }
  u64 gr = 0;
  const size_t goff = tid < 256 ? WS_GROW + (size_t)(chunk * 256 + tid) * 8 : WS_GCOL + (size_t)(g * 96 + (tid < 352 ? tid - 256 : 0)) * 8;
  const int wcs = ((wm & 1) ? wo1 : wo0) + (wm >> 1) * 8192;
  f32x4 accS = {0.f, 0.f, 0.f, 0.f};
  const bf16x8 ones = __builtin_bit_cast(bf16x8, u32x4{0x3f803f80u, 0x3f803f80u, 0x3f803f80u, 0x3f803f80u});
#define CSUM(kk) do { accS = mfma(wf[kk][5], ones, accS); asm volatile("" : "+v"(accS)); __builtin_amdgcn_sched_barrier(0); } while (0)
#define PREF(i)                                                                         \
  do {                                                                                  \
    const int id = ((bi - 4) * 2 + (i)) * 336 + (tid < 336 ? tid : 335);     \
    const char* pbase = (const char*)((bi - 4) * 2 + (i) < 38 ? a.wd : (bi - 4) * 2 + (i) < 47 ? a.cre : a.cim); \
    const int sub = (bi - 4) * 2 + (i) < 38 ? 0 : (bi - 4) * 2 + (i) < 47 ? 38 * 336 : 47 * 336; \
    int ln = (bi - 4) * 2 + (i) < 38 ? id : (id - sub) / 336 * 349 + (tid < 349 ? tid : 348); \
    const int lim = (bi - 4) * 2 + (i) < 38 ? 12543 : 3135;                             \
    ln = ln < lim ? ln : lim;                                                           \
    u32 keep;                                                                           \
    asm volatile("s_mov_b32 %0, m0\n\ts_mov_b32 m0, %3\n\ts_nop 0\n\tglobal_load_lds_dword %1, %2\n\ts_mov_b32 m0, %0" \
                 : "=&s"(keep) : "v"(ln * 128), "s"(pbase), "s"(lds0 + OFF_EP + 3584) : "memory"); \
    __builtin_amdgcn_sched_barrier(0);                                                  \
  } while (0)
#define TOUCH()                                                                         \
  do {                                                                                  \
    const int rt2 = 4 * xcd + ((bi - 4) & 3);                                           \
    const char* pbase = w == 7 ? (const char*)a.out : (const char*)ws;                  \
    const u32 po = w < 4 ? (chunk * 4 + w + (lane & 1)) * (KS2 * 4096)                  \
                 : w == 4 ? (rt2 + (lane & 1)) * (KS2 * 4096)                           \
                 : w == 7 ? (64 * rt2 + lane) * (DM * 4) : (u32)WS_GROW + (lane & 7) * 65536 + 61440; \
    u32 keep;                                                                           \
    asm volatile("s_mov_b32 %0, m0\n\ts_mov_b32 m0, %3\n\ts_nop 0\n\tglobal_load_lds_dword %1, %2 sc1\n\ts_mov_b32 m0, %0" \
                 : "=&s"(keep) : "v"(po), "s"(pbase), "s"(lds0 + OFF_EP + 3584) : "memory"); \
    __builtin_amdgcn_sched_barrier(0);                                                  \
  } while (0)
#define TR(o) __builtin_amdgcn_ds_read_tr16_b64_v4i16((LAS s16x4*)(sb + (o)))
#define WFRAG(o) __builtin_bit_cast(bf16x8, __builtin_shufflevector(TR(o), TR((o) + 512), 0, 1, 2, 3, 4, 5, 6, 7))
  bf16x8 xf[2][4], wf[2][6];
#define FR(s, kk, j)                                                                    \
  do {                                                                                  \
    char* sb = smem + ((s) % 3) * STAGE;                                                \
    if ((j) == 0) {                                                                     \
      xf[kk][0] = *(const bf16x8*)(sb + (kk ? xo1 : xo0));                              \
      wf[kk][0] = WFRAG(wo0 + kk * 4096); wf[kk][1] = WFRAG(wo1 + kk * 4096);           \
    }                                                                                   \
    if ((j) == 1) {                                                                     \
      wf[kk][2] = WFRAG(wo0 + kk * 4096 + 8192); wf[kk][3] = WFRAG(wo1 + kk * 4096 + 8192); \
      wf[kk][4] = *(const bf16x8*)(sb + (kk ? bo1 : bo0));                              \
    }                                                                                   \
    if ((j) == 2) {                                                                     \
      _Pragma("unroll") for (int m = 1; m < 4; ++m)                                     \
          xf[kk][m] = *(const bf16x8*)(sb + (kk ? xo1 : xo0) + m * 2048);               \
      wf[kk][5] = WFRAG(wcs + kk * 4096);                                               \
    }                                                                                   \
  } while (0)
#define FRAGS(s, kk) do { FR(s, kk, 0); FR(s, kk, 1); FR(s, kk, 2); } while (0)
#define MM(kk, m)                                                                       \
  do {                                                                                  \
    accG[0][m] = mfma(wf[kk][0], xf[kk][m], accG[0][m]);                                \
    accG[1][m] = mfma(wf[kk][1], xf[kk][m], accG[1][m]);                                \
    accU[0][m] = mfma(wf[kk][2], xf[kk][m], accU[0][m]);                                \
    accU[1][m] = mfma(wf[kk][3], xf[kk][m], accU[1][m]);                                \
    accB[m] = mfma(xf[kk][m], wf[kk][4], accB[m]);                                      \
    _Pragma("unroll") for (int i_ = 0; i_ < 5; ++i_) {                                  \
      __builtin_amdgcn_sched_group_barrier(0x008, 1, 0);                                \
      __builtin_amdgcn_sched_group_barrier(0x002, 1, 0);                                \
      __builtin_amdgcn_sched_group_barrier(0x100, 1, 0);                                \
    }                                                                                   \
    asm volatile("" : "+v"(accG[0][m]), "+v"(accG[1][m]), "+v"(accU[0][m]), "+v"(accU[1][m]), "+v"(accB[m])); \
    __builtin_amdgcn_sched_barrier(0);                                                  \
  } while (0)
#define PIECE(s, i)                                                                     \
  do {                                                                                  \
    if ((s) >= 2 && (s) < 7 && !ABL_NOLOAD) {                                                          \
      const u32 sb = lds0 + ((s) % 3) * STAGE;                                          \
      if ((i) < 4) glds((const char*)a.x + (s) * 128, xs[(i) & 3], sb + (4 * w + (i)) * 1024); \
      if ((i) == 4) glds((const char*)a.wg + (s) * (64 * FF * 2), woff, sb + OFF_G + w * 1024);  \
      if ((i) == 5) glds((const char*)a.wu + (s) * (64 * FF * 2), woff, sb + OFF_U + w * 1024);  \
      if ((i) == 6) glds_lo(bbase + (s) * 128, boff, sb + OFF_B + w * 512);    \
      __builtin_amdgcn_sched_barrier(0);                                                \
    }                                                                                   \
  } while (0)
#define BAR() do { asm volatile("s_waitcnt lgkmcnt(0)" ::: "memory"); __builtin_amdgcn_s_barrier(); asm volatile("" ::: "memory"); __builtin_amdgcn_sched_barrier(0); } while (0)
#define STEP(s, nv)                                                                     \
  do {                                                                                  \
    FR(s, 1, 0); MM(0, 0); if ((s) == 0) PIECE(2, 0); PIECE((s) + 2, 4);                \
    FR(s, 1, 1); MM(0, 1); if ((s) == 0) PIECE(2, 1); PIECE((s) + 2, 5);                \
    FR(s, 1, 2); MM(0, 2); if ((s) == 0) PIECE(2, 2); PIECE((s) + 2, 6);                \
    MM(0, 3); if ((s) == 0) PIECE(2, 3); CSUM(0);                                       \
    WAITV(nv);                                                                          \
    if ((s) == 5) {                                                                     \
      _Pragma("unroll 1") for (u32 spins = 0; spins < SPIN_CAP; ++spins) {              \
        if (__all((u32)(gr >> 32) == tag)) break;                                       \
        __builtin_amdgcn_s_sleep(2);                                                    \
        gr = get_gran(ws, goff);                                                        \
      }                                                                                 \
      __builtin_amdgcn_s_waitcnt(0x0f70);                                               \
      if (tid < 256) rowst[tid] = f32x2{bf2f((u32)gr & 0xffffu), bfhi((u32)gr)};        \
      else if (tid < 352) colsum[tid - 128] = __uint_as_float((u32)gr);          \
    }                                                                                   \
    BAR();                                                                              \
    if ((s) == PREF0) PREF(0); if ((s) == PREF1) PREF(1);                                                 \
    if ((s) == 2 && !ABL_NOTOUCH) TOUCH();                                              \
    if ((s) == 4) {                                                                     \
      gr = get_gran(ws, goff);                                                          \
      if (tid == 0) __hip_atomic_store((GAS u32*)(ws + WS_ARR) + blockIdx.x, tag, RLX_AGENT);   \
      __builtin_amdgcn_sched_barrier(0);                                                \
    }                                                                                   \
    FR((s) + 1, 0, 0); MM(1, 0); PIECE((s) + 3, 0); FR((s) + 1, 0, 1); MM(1, 1); PIECE((s) + 3, 1); \
    FR((s) + 1, 0, 2); MM(1, 2); PIECE((s) + 3, 2); MM(1, 3); PIECE((s) + 3, 3); CSUM(1); \
  } while (0)
  f32x2* rowst = (f32x2*)(smem + OFF_EP);
  float* colsum = (float*)(smem + OFF_EP + 2048);
  f32x2* wtot = (f32x2*)(smem + OFF_EP + 3072);
  STAMP(0, 3);
  WAITV(7); BAR(); STAMP(0, 2); STAMP(0, 14); FRAGS(0, 0); __builtin_amdgcn_sched_barrier(0);
  STEP(0, 7); STEP(1, 7); STEP(2, 7); STEP(3, 7); STEP(4, 7); STEP(5, 0);
  FRAGS(6, 1);
#pragma unroll
  for (int kk = 0; kk < 2; ++kk)
#pragma unroll
    for (int m = 0; m < 4; ++m) accB[m] = mfma(xf[kk][m], wf[kk][4], accB[m]);
  __builtin_amdgcn_sched_barrier(0);
  CSUM(0); CSUM(1);
  STAMP(0, 5); STAMP(0, 15);
#define MMGU(kk)                                                                        \
  _Pragma("unroll") for (int m = 0; m < 4; ++m) {                                       \
    accG[0][m] = mfma(wf[kk][0], xf[kk][m], accG[0][m]);                                \
    accG[1][m] = mfma(wf[kk][1], xf[kk][m], accG[1][m]);                                \
    accU[0][m] = mfma(wf[kk][2], xf[kk][m], accU[0][m]);                                \
    accU[1][m] = mfma(wf[kk][3], xf[kk][m], accU[1][m]);                                \
  }
  float xre[8], xim[8];
#pragma unroll
  for (int i = 0; i < 8; ++i) {
    const float flo = accB[i >> 2][i & 3], fhi = accB[2 + (i >> 2)][i & 3];
    const int lo = __float_as_int(flo), hi = __float_as_int(fhi);
    xre[i] = __int_as_float(__builtin_amdgcn_update_dpp(lo, hi, 0x128, 0xf, 0xc, false));
    xim[i] = __int_as_float(__builtin_amdgcn_update_dpp(hi, lo, 0x128, 0xf, 0x3, false));
  }
  asm volatile("s_nop 1" : "+v"(xre[0]), "+v"(xre[1]), "+v"(xre[2]), "+v"(xre[3]), "+v"(xre[4]), "+v"(xre[5]), "+v"(xre[6]), "+v"(xre[7]),
                           "+v"(xim[0]), "+v"(xim[1]), "+v"(xim[2]), "+v"(xim[3]), "+v"(xim[4]), "+v"(xim[5]), "+v"(xim[6]), "+v"(xim[7]));
  __builtin_amdgcn_sched_barrier(0);

  const int seg = lane >> 3, hf = seg & 1, ch = 8 * wn + (lane & 7);
  cf h[8];
  const f32x4 lc = *(const f32x4*)(colsum + 160 + ch * 4);
  const cf lam = {lc[0], lc[1]};
  const cf t2 = cmul(lam, lam), t4 = cmul(t2, t2), l8 = cmul(t4, t4), l16 = cmul(l8, l8), l32 = cmul(l16, l16);
  const cf l64 = cmul(l32, l32);
  {
    const float csr = colsum[128 + ch], csi = colsum[144 + ch];
#pragma unroll
    for (int i = 0; i < 8; ++i) {
      const float br = xre[i], bi = xim[i];
      const f32x2 st = rowst[64 * wm + 8 * seg + i];
      const float rs = __builtin_amdgcn_rcpf(st[1]);
      const cf u = {fmaf(-st[0], csr, br) * rs, fmaf(-st[0], csi, bi) * rs};
      h[i] = i ? cfma(lam, h[i - 1], u) : u;
    }
  }
  cf e = h[7];
  {
    cf p = {__shfl_up(e.r, 8), __shfl_up(e.i, 8)};
    e = csel(seg >= 1, cfma(l8, p, e), e);
    p = {__shfl_up(e.r, 16), __shfl_up(e.i, 16)};
    e = csel(seg >= 2, cfma(l16, p, e), e);
    p = {__shfl_up(e.r, 32), __shfl_up(e.i, 32)};
    e = csel(seg >= 4, cfma(l32, p, e), e);
  }
  cf pre = {__shfl_up(e.r, 8), __shfl_up(e.i, 8)};
  pre = csel(seg >= 1, pre, cf{0.f, 0.f});
  STAMP(0, 13);
  if (seg == 7) wtot[wm * 16 + ch] = f32x2{e.r, e.i};
  if (r == 0) *(f32x4*)(colsum + 64 * (wm >> 1) + 32 * wn + 16 * (wm & 1) + 4 * q) = accS;
  BAR();
  cf cw = {0.f, 0.f};
#pragma unroll
  for (int k = 0; k < 3; ++k) {
    const f32x2 v = wtot[k * 16 + ch];
    if (k < wm) cw = cfma(l64, cw, cf{v[0], v[1]});
  }
  const size_t aoff = WS_GAGG + (size_t)(g * 8 * 16) * 16;
  if (wm == 3 && seg == 7 && chunk < 7) {
    const cf agg = cfma(l64, cw, e);
    put_gran(ws, aoff + (size_t)(chunk * 16 + ch) * 16, tag, __float_as_uint(agg.r));
    put_gran(ws, aoff + (size_t)(chunk * 16 + ch) * 16 + 8, tag, __float_as_uint(agg.i));
  }
  STAMP(0, 8);
  __builtin_amdgcn_sched_barrier(0);
  if (PRIO) __builtin_amdgcn_s_setprio(3);
  MMGU(0);
  MMGU(1);
  __builtin_amdgcn_sched_barrier(0);
  char* a2 = ws + WS_A2 + (size_t)(chunk * 4 + wm) * KS2 * 4096;
  {
    f32x4 csg[2], csu[2];
#pragma unroll
    for (int nb = 0; nb < 2; ++nb) {
      csg[nb] = *(const f32x4*)(colsum + 32 * wn + 16 * nb + 4 * q);
      csu[nb] = *(const f32x4*)(colsum + 64 + 32 * wn + 16 * nb + 4 * q);
    }
#pragma unroll
    for (int m = 0; m < 4; ++m) {
      const f32x2 st = rowst[64 * wm + 16 * (r >> 2) + 4 * m + (r & 3)];
      const float c1 = -1.44269504f * __builtin_amdgcn_rcpf(st[1]), v2 = st[1] * st[1];
      u32x4 o;
#pragma unroll
      for (int nb = 0; nb < 2; ++nb) {
        const f32x4 gp = accG[nb][m] - st[0] * csg[nb], up = accU[nb][m] - st[0] * csu[nb];
        const f32x4 arg = gp * c1;
        f32x4 ex;
#pragma unroll
        for (int j = 0; j < 4; ++j) ex[j] = __builtin_amdgcn_exp2f(arg[j]);
        const f32x4 dn = ex * v2 + v2;
        f32x4 rc;
#pragma unroll
        for (int j = 0; j < 4; ++j) rc[j] = __builtin_amdgcn_rcpf(dn[j]);
        const f32x4 hv = gp * up * rc;
        o[2 * nb] = pk2(hv[0], hv[1]); o[2 * nb + 1] = pk2(hv[2], hv[3]);
      }
      st16(a2, (2 * g + wn) * 4096 + m * 1024 + lane * 16, o);
      __builtin_amdgcn_sched_barrier(0);
    }
  }
  STAMP(0, 9);
  const bool need = seg < chunk;
  const size_t po = aoff + (size_t)((need ? seg : 0) * 16 + ch) * 16;
  u64 g0 = get_gran(ws, po), g1 = get_gran(ws, po + 8);
  if (PRIO) __builtin_amdgcn_s_setprio(0);
  __builtin_amdgcn_sched_barrier(0);
  const cf one = {1.f, 0.f};
  const cf l128 = cmul(l64, l64), l256 = cmul(l128, l128), l512 = cmul(l256, l256), l1024 = cmul(l512, l512);
  const cf pseg = cmul(cmul(csel(seg & 1, l8, one), csel(seg & 2, l16, one)), csel(seg & 4, l32, one));
  const cf c2 = {2.f * lc[2], 2.f * lc[3]};
  cf s = cfma(pseg, cw, pre);
#pragma unroll
  for (int i = 0; i < 8; ++i) { s = cmul(lam, s); h[i] = cmul(c2, cf{h[i].r + s.r, h[i].i + s.i}); }
  GAS u32* myflag = (GAS u32*)(ws + WS_FLG + (size_t)(chunk * 4 + wm) * 512) + 2 * g + wn;
  const int nb_ = chunk - 1 - seg;
  cf pw = cmul(cmul(csel(nb_ & 1, l256, one), csel(nb_ & 2, l512, one)), csel(nb_ & 4, l1024, one));
  pw = csel(need, pw, cf{0.f, 0.f});
  cf pin = cmul(cmul(cmul(csel(wm & 1, l64, one), csel(wm & 2, l128, one)), pseg), c2);
  if (PIN) {
#pragma unroll
    for (int i = 0; i < 8; ++i) asm volatile("" : "+v"(h[i].r), "+v"(h[i].i));
    asm volatile("" : "+v"(pw.r), "+v"(pw.i), "+v"(pin.r), "+v"(pin.i));
  }
  STAMP(0, 6);
  auto finish = [&]() __attribute__((always_inline)) {
    asm volatile("s_waitcnt vmcnt(0)" : "+v"(g0), "+v"(g1) :: "memory");
    if (lane == 0) __hip_atomic_store(myflag, tag, RLX_AGENT);
    STAMP(0, 7);
    {
      const char* pw2 = (const char*)a.wd + w * 16 * (DM * 2) + ((bi - 4) >> 2) * 128;
      const u32 wo2 = (lane >> 3) * (DM * 2) + 16 * ((lane & 7) ^ (((lane >> 4) & 3) << 1));
#pragma unroll
      for (int k = 0; k < 2 * NPRE; ++k)
        glds(pw2 + ((k >> 1) * 128 + (k & 1) * 8) * (DM * 2), wo2, lds0 + (k >> 1) * SLOT + 16384 + w * 2048 + (k & 1) * 1024);
    }
    if (chunk > 0) {
      if (!__all((u32)(g0 >> 32) == tag && (u32)(g1 >> 32) == tag)) {
#pragma unroll 1
        for (u32 spins = 0; spins < SPIN_CAP; ++spins) {
          g0 = get_gran(ws, po); g1 = get_gran(ws, po + 8);
          if (__all((u32)(g0 >> 32) == tag && (u32)(g1 >> 32) == tag)) break;
        }
      }
      STAMP(0, 10);
      cf v = cmul(pw, cf{__uint_as_float((u32)g0), __uint_as_float((u32)g1)});
      v.r += dpp<0x128>(v.r); v.i += dpp<0x128>(v.i);
#pragma unroll
      for (int o = 16; o <= 32; o <<= 1) { v.r += __shfl_xor(v.r, o); v.i += __shfl_xor(v.i, o); }
      v = cmul(pin, v);
#pragma unroll
      for (int i = 0; i < 8; ++i) { v = cmul(lam, v); h[i].r += v.r; h[i].i += v.i; }
    }
    u32 o[8];
#pragma unroll
    for (int i = 0; i < 8; ++i) o[i] = pk2(h[i].r, -h[i].i);
    const bool b0 = lane & 1, b1 = lane & 2;
#pragma unroll
    for (int k = 0; k < 8; k += 2) {
      const u32 rc = dppu<0xB1>(b0 ? o[k] : o[k + 1]);
      if (b0) o[k] = rc; else o[k + 1] = rc;
    }
#pragma unroll
    for (int k = 0; k < 8; ++k)
      if (!(k & 2)) {
        const u32 rc = dppu<0x4E>(b1 ? o[k] : o[k + 2]);
        if (b1) o[k] = rc; else o[k + 2] = rc;
      }
    const u32 ho = (56 + g) * 4096 + (ch >> 2) * 256 + (4 * q + (lane & 3)) * 16 + hf * 2048;
#pragma unroll
    for (int k = 0; k < 2; ++k) {
      const u32* p = o + 4 * k;
      st16(a2, ho + k * 1024,
           u32x4{__builtin_amdgcn_perm(p[1], p[0], 0x05040100u), __builtin_amdgcn_perm(p[3], p[2], 0x05040100u),
                 __builtin_amdgcn_perm(p[1], p[0], 0x07060302u), __builtin_amdgcn_perm(p[3], p[2], 0x07060302u)});
    }
    STAMP(0, 11);
    STAMP_OUT(0);
  };
  phase2(a, smem, tag, bi - 4, myflag, finish);
}

extern "C" void kernel_launch(void* const* d_in, const int* in_sizes, int n_in, void* d_out, int out_size,
                              void* d_ws, size_t ws_size, hipStream_t stream) {
  K1Args a;
  a.x = (const u16*)d_in[0]; a.lre = (const u16*)d_in[1]; a.lim = (const u16*)d_in[2];
  a.bre = (const u16*)d_in[3]; a.bim = (const u16*)d_in[4]; a.cre = (const u16*)d_in[5];
  a.cim = (const u16*)d_in[6]; a.ldt = (const u16*)d_in[7]; a.wg = (const u16*)d_in[10];
  a.wu = (const u16*)d_in[11]; a.wd = (const u16*)d_in[12]; a.ws = (char*)d_ws;
  a.dsk = (const u16*)d_in[8]; a.out = (float*)d_out;
  k1<<<256, 512, 0, stream>>>(a);
}
```
